```python
import torch

def _softmax(x, axis=-1):
    return torch.softmax(x, dim=axis)
def _gelu(x):
    return torch.nn.functional.gelu(x, approximate="tanh")
def _silu(x):
    return torch.nn.functional.silu(x)
def _sigmoid(x):
    return torch.sigmoid(x)
def _layernorm(x, axis=-1, eps=1e-5):
    m = x.mean(dim=axis, keepdim=True)
    v = x.var(dim=axis, keepdim=True, unbiased=False)
    return (x - m) / torch.sqrt(v + eps)
def _scatter_add(tgt, idx, src, axis=0):
    return tgt.clone().index_add_(axis, idx, src)
def _segment_sum(src, seg, n):
    out = torch.zeros((n,) + src.shape[1:], dtype=src.dtype)
    return out.index_add_(0, seg, src)
def _pad(x, pw):
    flat = tuple(v for pair in reversed(pw) for v in pair)
    return torch.nn.functional.pad(x, flat)

kernel_name = 'gated_mix__linear_kernel_hedgehog__mixer_channel_mlp__prompt'

def setup_inputs():

    _w = (lambda a: (lambda t: t.to(torch.bfloat16) if t.is_floating_point() else t)(torch.as_tensor(a)))
    def _A_setup():
        B, S, D, H = 1, 2048, 512, 32
        Dh = D // H
        return {
            "x": _w((lambda *s: torch.randn(*s, dtype=torch.bfloat16) * 0.05)(B, S, D)),
            "wqkv": _w((lambda *s: torch.randn(*s, dtype=torch.bfloat16) * 0.8)(D, 3 * D)),
            "w_phi": _w((lambda *s: torch.randn(*s, dtype=torch.bfloat16) * 0.8)(H, Dh, Dh) * (Dh ** -0.5)),
            "wo": _w((lambda *s: torch.randn(*s, dtype=torch.bfloat16) * 0.1)(D, D)),
        }
    _A_in = _A_setup()

    def _B_setup():
        B, S, D, F = 1, 2048, 512, 1376
        return {
            "x": _w((lambda *s: torch.randn(*s, dtype=torch.bfloat16) * 0.05)(B, S, D)),
            "w1": _w((lambda *s: torch.randn(*s, dtype=torch.bfloat16) * 0.8)(D, F) * (D ** -0.5)),
            "b1": _w((lambda *s: torch.randn(*s, dtype=torch.bfloat16) * 0.4)(F)),
            "w2": _w((lambda *s: torch.randn(*s, dtype=torch.bfloat16) * 0.05)(F, D) * (F ** -0.5)),
            "b2": _w((lambda *s: torch.randn(*s, dtype=torch.bfloat16) * 0.05)(D)),
        }
    _B_in = _B_setup()

    return {
        "A_x": _A_in["x"],
        "A_wqkv": _A_in["wqkv"],
        "A_w_phi": _A_in["w_phi"],
        "A_wo": _A_in["wo"],
        "B_x": _B_in["x"],
        "B_w1": _B_in["w1"],
        "B_b1": _B_in["b1"],
        "B_w2": _B_in["w2"],
        "B_b2": _B_in["b2"],
        "gate_w": _w((lambda *s: torch.randn(*s, dtype=torch.bfloat16) * 0.05)(512) * 16.0),
    }

def reference(A_x, A_wqkv, A_w_phi, A_wo, B_x, B_w1, B_b1, B_w2, B_b2, gate_w):
    def _A_ref(x, wqkv, w_phi, wo):
        B, S, D = x.shape
        H = 32; Dh = D // H
        qkv = (lambda x, *d: x.permute(*d))((x @ wqkv).reshape(B, S, 3, H, Dh), 2, 0, 3, 1, 4)
        q, k, v = qkv[0], qkv[1], qkv[2]
        pq = torch.einsum("bhsd,hdf->bhsf", q, w_phi)
        pk = torch.einsum("bhsd,hdf->bhsf", k, w_phi)
        phi_q = torch.concatenate([_softmax(pq, axis=-1), _softmax(-pq, axis=-1)], axis=-1)
        phi_k = torch.concatenate([_softmax(pk, axis=-1), _softmax(-pk, axis=-1)], axis=-1)
        kv = torch.einsum("bhsf,bhsd->bhfd", phi_k, v)
        z  = torch.einsum("bhsf->bhf", phi_k)
        num = torch.einsum("bhqf,bhfd->bhqd", phi_q, kv)
        den = torch.einsum("bhqf,bhf->bhq", phi_q, z)[..., None] + 1e-6
        o = num / den
        return (lambda x, *d: x.permute(*d))(o, 0, 2, 1, 3).reshape(B, S, D) @ wo

    def _B_ref(x, w1, b1, w2, b2):
        y = _layernorm(x, axis=-1)
        h = _gelu(torch.einsum("bsd,df->bsf", y, w1) + b1)
        return torch.einsum("bsf,fd->bsd", h, w2) + b2

    x = A_x
    g = _sigmoid((x * gate_w).sum(-1, keepdims=True))
    a = _A_ref(x, A_wqkv, A_w_phi, A_wo)
    b = _B_ref(x, B_w1, B_b1, B_w2, B_b2)
    return g * a + (1 - g) * b
```

```cpp
#include <hip/hip_runtime.h>
#include <hip/hip_bf16.h>
typedef unsigned short u16;
__device__ __forceinline__ float bf2f(u16 b){ return __uint_as_float(((unsigned)b)<<16); }
__device__ __forceinline__ u16 f2bf(float f){ unsigned u=__float_as_uint(f); u+=0x7fffu+((u>>16)&1u); return (u16)(u>>16); }
__device__ __forceinline__ float rbf(float f){ return bf2f(f2bf(f)); }
constexpr int S=2048,D=512,H=32,DH=16,F=1376;

__global__ void k_qkv(const u16* x,const u16* w,float* qkv){
  int n=blockIdx.x*256+threadIdx.x; int m=blockIdx.y;
  float a=0; for(int k=0;k<D;k++) a+=bf2f(x[m*D+k])*bf2f(w[k*1536+n]);
  qkv[m*1536+n]=rbf(a);
}
__global__ void k_phi(const float* qkv,const u16* wphi,float* phi){
  int idx=blockIdx.x*256+threadIdx.x;
  int h=idx%H; int wh=(idx/H)%2; int s=idx/(2*H);
  const float* q=qkv+s*1536+wh*512+h*16;
  float p[16]; float mx=-1e30f,mn=1e30f;
  for(int f=0;f<16;f++){ float a=0; for(int d=0;d<16;d++) a+=q[d]*bf2f(wphi[h*256+d*16+f]); p[f]=rbf(a); mx=fmaxf(mx,p[f]); mn=fminf(mn,p[f]); }
  float s1=0,s2=0; for(int f=0;f<16;f++){ s1+=expf(p[f]-mx); s2+=expf(mn-p[f]); }
  float* o=phi+(((size_t)wh*S+s)*H+h)*32;
  for(int f=0;f<16;f++){ o[f]=rbf(expf(p[f]-mx)/s1); o[16+f]=rbf(expf(mn-p[f])/s2); }
}
__global__ void k_kv(const float* qkv,const float* phi,float* kv){
  int idx=blockIdx.x*256+threadIdx.x; if(idx>=H*32*17) return;
  int d=idx%17; int f=(idx/17)%32; int h=idx/(17*32);
  float a=0;
  for(int s=0;s<S;s++){ float pk=phi[(((size_t)1*S+s)*H+h)*32+f]; a+= d<16? pk*qkv[s*1536+1024+h*16+d] : pk; }
  kv[idx]=rbf(a);
}
__global__ void k_o(const float* phi,const float* kv,float* o){
  int idx=blockIdx.x*256+threadIdx.x; int d=idx%16; int h=(idx/16)%H; int s=idx/512;
  const float* pq=phi+(((size_t)0*S+s)*H+h)*32;
  float num=0,den=0; for(int f=0;f<32;f++){ num+=pq[f]*kv[(h*32+f)*17+d]; den+=pq[f]*kv[(h*32+f)*17+16]; }
  den=rbf(rbf(den)+1e-6f);
  o[idx]=rbf(rbf(num)/den);
}
__global__ void k_ln(const u16* x,const u16* gw,float* y,float* g){
  int m=blockIdx.x; int t=threadIdx.x; float s=0,gs=0;
  for(int k=t;k<D;k+=64){ float v=bf2f(x[m*D+k]); s+=v; gs+=v*bf2f(gw[k]); }
  for(int o=32;o;o>>=1){ s+=__shfl_xor(s,o); gs+=__shfl_xor(gs,o);} float mean=s/D; float vv=0;
  for(int k=t;k<D;k+=64){ float v=bf2f(x[m*D+k])-mean; vv+=v*v; }
  for(int o=32;o;o>>=1) vv+=__shfl_xor(vv,o);
  float r=rsqrtf(vv/D+1e-5f);
  for(int k=t;k<D;k+=64) y[m*D+k]=rbf((bf2f(x[m*D+k])-mean)*r);
  if(t==0) g[m]=1.f/(1.f+expf(-gs));
}
__global__ void k_h(const float* y,const u16* w1,const u16* b1,float* hh){
  int n=blockIdx.x*256+threadIdx.x; int m=blockIdx.y; if(n>=F) return;
  float a=0; for(int k=0;k<D;k++) a+=y[m*D+k]*bf2f(w1[k*F+n]);
  a=rbf(rbf(a)+bf2f(b1[n]));
  float t=tanhf(0.7978845608f*(a+0.044715f*a*a*a));
  hh[m*F+n]=rbf(0.5f*a*(1.f+t));
}
__global__ void k_out(const float* o,const float* hh,const u16* wo,const u16* w2,const u16* b2,const float* g,u16* out){
  int n=blockIdx.x*256+threadIdx.x; int m=blockIdx.y;
  float a=0; for(int k=0;k<D;k++) a+=o[m*D+k]*bf2f(wo[k*D+n]);
  float b=0; for(int k=0;k<F;k++) b+=hh[m*F+k]*bf2f(w2[k*D+n]);
  b+=bf2f(b2[n]);
  float gg=g[m];
  out[m*D+n]=f2bf(gg*a+(1.f-gg)*b);
}
extern "C" void kernel_launch(void* const* d_in, const int* in_sizes, int n_in, void* d_out, int out_size, void* d_ws, size_t ws_size, hipStream_t stream){
  const u16* x=(const u16*)d_in[0]; const u16* wqkv=(const u16*)d_in[1]; const u16* wphi=(const u16*)d_in[2]; const u16* wo=(const u16*)d_in[3];
  const u16* w1=(const u16*)d_in[5]; const u16* b1=(const u16*)d_in[6]; const u16* w2=(const u16*)d_in[7]; const u16* b2=(const u16*)d_in[8]; const u16* gw=(const u16*)d_in[9];
  float* ws=(float*)d_ws;
  float* qkv=ws; float* phi=qkv+S*1536; float* kv=phi+(size_t)2*S*H*32; float* o=kv+H*32*17+64; float* y=o+S*D; float* g=y+S*D; float* hh=g+S;
  k_qkv<<<dim3(6,S),256,0,stream>>>(x,wqkv,qkv);
  k_phi<<<S*2*H/256,256,0,stream>>>(qkv,wphi,phi);
  k_kv<<<(H*32*17+255)/256,256,0,stream>>>(qkv,phi,kv);
  k_o<<<S*D/256,256,0,stream>>>(phi,kv,o);
  k_ln<<<S,64,0,stream>>>(x,gw,y,g);
  k_h<<<dim3(6,S),256,0,stream>>>(y,w1,b1,hh);
  k_out<<<dim3(2,S),256,0,stream>>>(o,hh,wo,w2,b2,g,(u16*)d_out);
}
```

```cpp
#include <hip/hip_runtime.h>
#include <hip/hip_bf16.h>
typedef unsigned short u16;
__device__ __forceinline__ float bf2f(u16 b){ return __uint_as_float(((unsigned)b)<<16); }
__device__ __forceinline__ u16 f2bf(float f){ unsigned u=__float_as_uint(f); u+=0x7fffu+((u>>16)&1u); return (u16)(u>>16); }
__device__ __forceinline__ float rbf(float f){ return bf2f(f2bf(f)); }
constexpr int S=2048,D=512,H=32,DH=16,F=1376;

__global__ void k_qkv(const u16* x,const u16* w,float* qkv){
  int n=blockIdx.x*256+threadIdx.x; int m=blockIdx.y;
  float a=0; for(int k=0;k<D;k++) a+=bf2f(x[m*D+k])*bf2f(w[k*1536+n]);
  qkv[m*1536+n]=rbf(a);
}
__global__ void k_phi(const float* qkv,const u16* wphi,float* phi){
  int idx=blockIdx.x*256+threadIdx.x;
  int h=idx%H; int wh=(idx/H)%2; int s=idx/(2*H);
  const float* q=qkv+s*1536+wh*512+h*16;
  float p[16]; float mx=-1e30f,mn=1e30f;
  for(int f=0;f<16;f++){ float a=0; for(int d=0;d<16;d++) a+=q[d]*bf2f(wphi[h*256+d*16+f]); p[f]=a; mx=fmaxf(mx,p[f]); mn=fminf(mn,p[f]); }
  float s1=0,s2=0; for(int f=0;f<16;f++){ s1+=expf(p[f]-mx); s2+=expf(mn-p[f]); }
  float* o=phi+(((size_t)wh*S+s)*H+h)*32;
  for(int f=0;f<16;f++){ o[f]=rbf(expf(p[f]-mx)/s1); o[16+f]=rbf(expf(mn-p[f])/s2); }
}
__global__ void k_kv(const float* qkv,const float* phi,float* kv){
  int idx=blockIdx.x*256+threadIdx.x; if(idx>=H*32*17) return;
  int d=idx%17; int f=(idx/17)%32; int h=idx/(17*32);
  float tot=0;
  for(int s0=0;s0<S;s0+=128){ float a=0;
  for(int s=s0;s<s0+128;s++){ float pk=phi[(((size_t)1*S+s)*H+h)*32+f]; a+= d<16? pk*qkv[s*1536+1024+h*16+d] : pk; }
  tot+=rbf(a);}
  kv[idx]=rbf(tot);
}
__global__ void k_o(const float* phi,const float* kv,float* o){
  int idx=blockIdx.x*256+threadIdx.x; int d=idx%16; int h=(idx/16)%H; int s=idx/512;
  const float* pq=phi+(((size_t)0*S+s)*H+h)*32;
  float num=0,den=0; for(int f=0;f<32;f++){ num+=pq[f]*kv[(h*32+f)*17+d]; den+=pq[f]*kv[(h*32+f)*17+16]; }
  den=den+1e-6f;
  o[idx]=rbf(num/den);
}
__global__ void k_ln(const u16* x,const u16* gw,float* y,float* g){
  int m=blockIdx.x; int t=threadIdx.x; float s=0,gs=0;
  for(int k=t;k<D;k+=64){ float v=bf2f(x[m*D+k]); s+=v; gs+=v*bf2f(gw[k]); }
  for(int o=32;o;o>>=1){ s+=__shfl_xor(s,o); gs+=__shfl_xor(gs,o);} float mean=s/D; float vv=0;
  for(int k=t;k<D;k+=64){ float v=bf2f(x[m*D+k])-mean; vv+=v*v; }
  for(int o=32;o;o>>=1) vv+=__shfl_xor(vv,o);
  float r=rsqrtf(vv/D+1e-5f);
  for(int k=t;k<D;k+=64) y[m*D+k]=(bf2f(x[m*D+k])-mean)*r;
  if(t==0) g[m]=1.f/(1.f+expf(-gs));
}
__global__ void k_h(const float* y,const u16* w1,const u16* b1,float* hh){
  int n=blockIdx.x*256+threadIdx.x; int m=blockIdx.y; if(n>=F) return;
  float a=0; for(int k=0;k<D;k++) a+=y[m*D+k]*bf2f(w1[k*F+n]);
  a=a+bf2f(b1[n]);
  float t=tanhf(0.7978845608f*(a+0.044715f*a*a*a));
  hh[m*F+n]=rbf(0.5f*a*(1.f+t));
}
__global__ void k_out(const float* o,const float* hh,const u16* wo,const u16* w2,const u16* b2,const float* g,u16* out){
  int n=blockIdx.x*256+threadIdx.x; int m=blockIdx.y;
  float a=0; for(int k=0;k<D;k++) a+=o[m*D+k]*bf2f(wo[k*D+n]);
  float b=0; for(int k=0;k<F;k++) b+=hh[m*F+k]*bf2f(w2[k*D+n]);
  b+=bf2f(b2[n]);
  float gg=g[m];
  out[m*D+n]=f2bf(gg*a+(1.f-gg)*b);
}
extern "C" void kernel_launch(void* const* d_in, const int* in_sizes, int n_in, void* d_out, int out_size, void* d_ws, size_t ws_size, hipStream_t stream){
  const u16* x=(const u16*)d_in[0]; const u16* wqkv=(const u16*)d_in[1]; const u16* wphi=(const u16*)d_in[2]; const u16* wo=(const u16*)d_in[3];
  const u16* w1=(const u16*)d_in[5]; const u16* b1=(const u16*)d_in[6]; const u16* w2=(const u16*)d_in[7]; const u16* b2=(const u16*)d_in[8]; const u16* gw=(const u16*)d_in[9];
  float* ws=(float*)d_ws;
  float* qkv=ws; float* phi=qkv+S*1536; float* kv=phi+(size_t)2*S*H*32; float* o=kv+H*32*17+64; float* y=o+S*D; float* g=y+S*D; float* hh=g+S;
  k_qkv<<<dim3(6,S),256,0,stream>>>(x,wqkv,qkv);
  k_phi<<<S*2*H/256,256,0,stream>>>(qkv,wphi,phi);
  k_kv<<<(H*32*17+255)/256,256,0,stream>>>(qkv,phi,kv);
  k_o<<<S*D/256,256,0,stream>>>(phi,kv,o);
  k_ln<<<S,64,0,stream>>>(x,gw,y,g);
  k_h<<<dim3(6,S),256,0,stream>>>(y,w1,b1,hh);
  k_out<<<dim3(2,S),256,0,stream>>>(o,hh,wo,w2,b2,g,(u16*)d_out);
}
```

```cpp
#include <hip/hip_runtime.h>
#include <hip/hip_bf16.h>

typedef unsigned long long u64;
typedef unsigned v4u __attribute__((ext_vector_type(4)));
typedef unsigned v2u __attribute__((ext_vector_type(2)));
typedef float f32x4 __attribute__((ext_vector_type(4)));
typedef float f32x2 __attribute__((ext_vector_type(2)));
typedef __bf16 bf16x8 __attribute__((ext_vector_type(8)));
typedef __bf16 bf16x2 __attribute__((ext_vector_type(2)));
typedef short s16x4 __attribute__((ext_vector_type(4)));
#define GAS __attribute__((address_space(1)))
#define LAS __attribute__((address_space(3)))
#define DI __device__ __forceinline__
#define RLX_AGENT __ATOMIC_RELAXED, __HIP_MEMORY_SCOPE_AGENT

#define STAMPS 0

namespace gm {
constexpr int S = 2048, D = 512, F = 1376;
constexpr unsigned WS_E = 0, WS_KVF = 4096, WS_HF = 12288, WS_OF = 20480, WS_G = 32768, WS_ZP = 40960, WS_KVP = 131072,
                   WS_ACT = 2u << 20, ACT_BYTES = 16u * 4 * 60 * 2048, WS_END = WS_ACT + ACT_BYTES, WS_STAMP = 16u << 20;
constexpr int SLOT = 40960, WOFF = 16384, PAGE = 16384;
constexpr int IMG = 73728;
constexpr int KVR = 9 * PAGE;
constexpr int KVR_ZERO = KVR + 4 * 17 * 64;
constexpr int STAT = 7 * PAGE;
constexpr int RED = 6 * PAGE;
constexpr int AUX_SC1 = 16;
constexpr unsigned SPIN_CAP = 1u << 22;

struct Params {
  const void *x, *wqkv, *wphi, *wo, *w1, *b1, *w2, *b2, *gw;
  void *out, *ws;
};

DI float asf(unsigned u) { return __builtin_bit_cast(float, u); }
DI unsigned asu(float f) { return __builtin_bit_cast(unsigned, f); }
DI bf16x2 asb2(unsigned u) { return __builtin_bit_cast(bf16x2, u); }
DI f32x4 mfma(v4u a, v4u b, f32x4 c) {
  return __builtin_amdgcn_mfma_f32_16x16x32_bf16(__builtin_bit_cast(bf16x8, a), __builtin_bit_cast(bf16x8, b), c, 0, 0, 0);
}
DI unsigned pk(float a, float b) { return __builtin_bit_cast(unsigned, __builtin_convertvector((f32x2){a, b}, bf16x2)); }
DI float bflo(unsigned u) { return __builtin_bit_cast(float, u << 16); }
DI float bfhi(unsigned u) { return __builtin_bit_cast(float, u & 0xffff0000u); }
DI v2u trr(LAS char* p) { return __builtin_bit_cast(v2u, __builtin_amdgcn_ds_read_tr16_b64_v4i16((LAS s16x4*)p)); }
DI v4u trr2(LAS char* p, int off2) { v2u a = trr(p), b = trr(p + off2); return (v4u){a[0], a[1], b[0], b[1]}; }
DI f32x2 pair16(float v) { auto r = __builtin_amdgcn_permlane16_swap(asu(v), asu(v), false, false); return (f32x2){asf(r[0]), asf(r[1])}; }
DI f32x2 pair32(float v) { auto r = __builtin_amdgcn_permlane32_swap(asu(v), asu(v), false, false); return (f32x2){asf(r[0]), asf(r[1])}; }
DI float sum16(float v) { const f32x2 r = pair16(v); return r[0] + r[1]; }
DI float sum32(float v) { const f32x2 r = pair32(v); return r[0] + r[1]; }
DI float max16(float v) { const f32x2 r = pair16(v); return fmaxf(r[0], r[1]); }
DI float min16(float v) { const f32x2 r = pair16(v); return fminf(r[0], r[1]); }
#define WAITV(n) asm volatile("s_waitcnt vmcnt(%0)" ::"n"(n) : "memory")
#define WAITL() asm volatile("s_waitcnt lgkmcnt(0)" ::: "memory")
#define BAR() do { asm volatile("" ::: "memory"); __builtin_amdgcn_s_barrier(); asm volatile("" ::: "memory"); } while (0)
#define LBAR() do { WAITL(); __builtin_amdgcn_s_barrier(); asm volatile("" ::: "memory"); } while (0)

DI void glds(__amdgpu_buffer_rsrc_t rs, LAS char* dst, unsigned voff, unsigned soff) {
  asm volatile("s_mov_b32 m0, %0\n\ts_nop 0\n\tbuffer_load_dwordx4 %1, %2, %3 offen lds" ::"s"((unsigned)(size_t)dst), "v"(voff), "s"(rs), "s"(soff) : "memory");
}
DI void dual_softmax(const f32x4& p0, const f32x4& p1, v4u& plus, v4u& minus) {
  float mx = fmaxf(fmaxf(fmaxf(p0[0], p0[1]), fmaxf(p0[2], p0[3])), fmaxf(fmaxf(p1[0], p1[1]), fmaxf(p1[2], p1[3])));
  float mn = fminf(fminf(fminf(p0[0], p0[1]), fminf(p0[2], p0[3])), fminf(fminf(p1[0], p1[1]), fminf(p1[2], p1[3])));
  mx = max16(mx); mn = min16(mn);
  constexpr float L2E = 1.4426950408889634f;
  const float mxs = -mx * L2E, mns = mn * L2E;
  float ep[8], em[8], sp = 0.f, sm = 0.f;
#pragma unroll
  for (int e = 0; e < 8; e++) {
    const float p = e < 4 ? p0[e & 3] : p1[e & 3];
    ep[e] = __builtin_amdgcn_exp2f(__builtin_fmaf(p, L2E, mxs));
    em[e] = __builtin_amdgcn_exp2f(__builtin_fmaf(p, -L2E, mns));
    sp += ep[e]; sm += em[e];
  }
  sp = __builtin_amdgcn_rcpf(sum16(sp)); sm = __builtin_amdgcn_rcpf(sum16(sm));
#pragma unroll
  for (int e = 0; e < 4; e++) { plus[e] = pk(ep[2 * e] * sp, ep[2 * e + 1] * sp); minus[e] = pk(em[2 * e] * sm, em[2 * e + 1] * sm); }
}
DI v4u pack8(const f32x4& a, const f32x4& b) { return (v4u){pk(a[0], a[1]), pk(a[2], a[3]), pk(b[0], b[1]), pk(b[2], b[3])}; }
DI void wait16(const GAS u64* line, u64 tag, int lane) {
#pragma nounroll
  for (unsigned spins = 0; spins < SPIN_CAP; ++spins) {
    const u64 v = __hip_atomic_load(line + (lane & 15), RLX_AGENT);
    if (__all(v == tag)) break;
    __builtin_amdgcn_s_sleep(1);
  }
  __builtin_amdgcn_fence(__ATOMIC_ACQUIRE, "wavefront");
}
DI void publish(GAS u64* flag, u64 tag, int lane) {
  WAITV(0);
  if (lane == 0) __hip_atomic_store(flag, tag, RLX_AGENT);
}

#define STAMP(i) do {} while (0)

__global__ void __launch_bounds__(512, 2) fused(Params p) {
  __shared__ __attribute__((aligned(16))) char smem[163840];
  LAS char* lds = (LAS char*)smem;
  const int tid = threadIdx.x, lane = tid & 63, w = __builtin_amdgcn_readfirstlane(tid >> 6);
  const int g = lane >> 4, j = lane & 15, q4 = (lane & 15) >> 2, p4 = lane & 3;
  const int tg = w >> 1, ch = w & 1;
  const int b = blockIdx.x, xcd = b & 7, idx = b >> 3;
  const int rt = (xcd >> 2) * 8 + (idx >> 2), c4 = idx & 3;
  const bool isM = c4 >= 2;
  const int grp = (xcd & 3) * 2 + (c4 & 1);
  const int u0 = grp < 3 ? 6 * grp : 18 + 5 * (grp - 3);
  const int rot = idx >> 2;
  const int panel = xcd * 4 + (idx >> 3), ct = idx & 7;
  STAMP(0);

  const __amdgpu_buffer_rsrc_t rs_ws = __builtin_amdgcn_make_buffer_rsrc(p.ws, 0, WS_END, 0x00020000);
  const __amdgpu_buffer_rsrc_t rs_x = __builtin_amdgcn_make_buffer_rsrc((void*)p.x, 0, S * D * 2, 0x00020000);
  const __amdgpu_buffer_rsrc_t rs_w = __builtin_amdgcn_make_buffer_rsrc((void*)(isM ? p.w1 : p.wqkv), 0, isM ? D * F * 2 : D * 1536 * 2, 0x00020000);
  const __amdgpu_buffer_rsrc_t rs_w2 = __builtin_amdgcn_make_buffer_rsrc((void*)p.w2, 0, F * D * 2, 0x00020000);
  const __amdgpu_buffer_rsrc_t rs_wo = __builtin_amdgcn_make_buffer_rsrc((void*)p.wo, 0, D * D * 2, 0x00020000);
  GAS char* wsp = (GAS char*)p.ws;

  v2u ev;
  asm volatile("global_load_dwordx2 %0, %1, off sc1" : "=v"(ev) : "v"(wsp + WS_E) : "memory");
  v4u aux[16]; v2u b1r[6]; v4u wph;

  unsigned xv[2], wv[3];
#pragma unroll
  for (int i = 0; i < 2; i++) { const int row = 8 * (2 * w + i) + (lane >> 3); xv[i] = (rt * 128 + row) * 1024 + (((lane & 7) ^ ((row >> 1) & 7)) * 16); }
#pragma unroll
  for (int i = 0; i < 3; i++) {
    const int n = (3 * w + i) * 64 + lane, k = n / 24, cp = n % 24;
    const int c = cp ^ (((k >> 1) & 1) | (((k >> 3) & 1) << 2));
    const int seg = c >> 3;
    wv[i] = isM ? k * 2752 + u0 * 64 + c * 16 : k * 3072 + (seg == 2 ? 0 : (seg + 1) * 1024) + grp * 128 + (c & 7) * 16;
  }
  const unsigned wstep = isM ? 64 * 2752 : 64 * 3072;
  const unsigned w2v = (16 * w + (lane >> 3)) * 1024 + ct * 128 + (((lane & 7) ^ ((lane >> 3) & 6)) * 16);
  auto stage = [&](int t) {
    const int kt = (t + rot) & 7;
    LAS char* dst = lds + (t & 3) * SLOT;
    glds(rs_x, dst + (2 * w) * 1024, xv[0], kt * 128);
    glds(rs_x, dst + (2 * w + 1) * 1024, xv[1], kt * 128);
#pragma unroll
    for (int i = 0; i < 3; i++) glds(rs_w, dst + WOFF + (3 * w + i) * 1024, wv[i], kt * wstep);
  };
  auto round_ld = [&](int r, int page) {
    LAS char* dst = lds + page * PAGE + w * 2048;
    if (r < 11) { glds(rs_w2, dst, w2v, r * 131072); glds(rs_w2, dst + 1024, w2v + 8192, r * 131072); }
    else { glds(rs_wo, dst, w2v, (r - 11) * 131072); glds(rs_wo, dst + 1024, w2v + 8192, (r - 11) * 131072); }
  };

  const int xb0 = (32 * tg + j) * 128 + ((g ^ (j >> 1)) * 16);
  const int sk = ((q4 >> 1) & 1) | ((g & 1) << 2);
  const int wrow = WOFF + (8 * g + q4) * 384 + 8 * (p4 & 1);
  const int wb0 = wrow + 16 * ((4 * ch + 2 * (p4 >> 1)) ^ sk), wb1 = wrow + 16 * ((4 * ch + 2 * (p4 >> 1) + 1) ^ sk);
  const int cs1 = tg < 2 ? tg + 4 : tg;
  const int cb0 = ((tg & 1) ? wb1 : wb0) + (tg >> 1) * 128, cb1 = ((cs1 & 1) ? wb1 : wb0) + (cs1 >> 1) * 128;

  f32x4 acc[2][6];
#pragma unroll
  for (int a = 0; a < 2; a++)
#pragma unroll
    for (int t = 0; t < 6; t++) acc[a][t] = (f32x4){0.f, 0.f, 0.f, 0.f};
  f32x4 cs[2] = {{0.f, 0.f, 0.f, 0.f}, {0.f, 0.f, 0.f, 0.f}}, ax = {0.f, 0.f, 0.f, 0.f};
  float ssq = 0.f;
  const v4u ones = {0x3F803F80u, 0x3F803F80u, 0x3F803F80u, 0x3F803F80u};

  auto kloop = [&](auto ism) {
    constexpr bool M = decltype(ism)::value;
    v4u xf[2][2], wf[2][6], cf[2][2];
    if (M) {
      const __amdgpu_buffer_rsrc_t rs_b1 = __builtin_amdgcn_make_buffer_rsrc((void*)p.b1, 0, F * 2, 0x00020000);
#pragma unroll
      for (int t = 0; t < 6; t++) {
        const unsigned off = ((u0 + 2 * (t >> 1) + ch) * 32 + 16 * (g >> 1) + 8 * (t & 1) + 4 * (g & 1)) * 2;
        asm volatile("buffer_load_dwordx2 %0, %1, %2, 0 offen" : "=v"(b1r[t]) : "v"(off), "s"(rs_b1) : "memory");
      }
    } else {
      const GAS char* a = (const GAS char*)p.wphi + (4 * grp + 2 * ch) * 512 + lane * 16;
      asm volatile("global_load_dwordx4 %0, %1, off" : "=v"(wph) : "v"(a) : "memory");
      const __amdgpu_buffer_rsrc_t rs_gw = __builtin_amdgcn_make_buffer_rsrc((void*)p.gw, 0, D * 2, 0x00020000);
#pragma unroll
      for (int s = 0; s < 16; s++)
        asm volatile("buffer_load_dwordx4 %0, %1, %2, %3 offen" : "=v"(aux[s]) : "v"(16 * g), "s"(rs_gw), "s"(((((s >> 1) + rot) & 7) * 64 + (s & 1) * 32) * 2) : "memory");
    }
    stage(0); stage(1); stage(2); stage(3);
    auto rd = [&](int t, int ks) {
      LAS char* sl = lds + (t & 3) * SLOT;
#pragma unroll
      for (int a = 0; a < 2; a++) xf[ks][a] = *(LAS v4u*)(sl + (xb0 ^ (ks * 64)) + a * 2048);
#pragma unroll
      for (int t6 = 0; t6 < 6; t6++) wf[ks][t6] = trr2(sl + ((t6 & 1) ? wb1 : wb0) + (t6 >> 1) * 128 + ks * 12288, 1536);
      if (M) { cf[ks][0] = trr2(sl + cb0 + ks * 12288, 1536); cf[ks][1] = trr2(sl + cb1 + ks * 12288, 1536); }
    };
    auto mm = [&](int ks, int t0, int t1) {
#pragma unroll
      for (int t6 = t0; t6 < t1; t6++)
#pragma unroll
        for (int a = 0; a < 2; a++) acc[a][t6] = mfma(wf[ks][t6], xf[ks][a], acc[a][t6]);
    };
    auto extra = [&](int i, int ks) {
      const v4u xs = ch ? xf[ks][1] : xf[ks][0];
      if (M) {
        cs[0] = mfma(cf[ks][0], ones, cs[0]); cs[1] = mfma(cf[ks][1], ones, cs[1]);
        ax = mfma(ones, xs, ax);
#pragma unroll
        for (int e = 0; e < 4; e++) ssq = __builtin_amdgcn_fdot2_f32_bf16(asb2(xs[e]), asb2(xs[e]), ssq, false);
        asm volatile("" : "+v"(cs[0]), "+v"(cs[1]), "+v"(ssq));
      } else ax = mfma(aux[2 * i + ks], xs, ax);
      asm volatile("" : "+v"(ax));
    };
    STAMP(21);
    WAITV(15);
    asm volatile("" : "+v"(ev));
    if (M) {
#pragma unroll
      for (int t = 0; t < 6; t++) asm volatile("" : "+v"(b1r[t]));
    } else {
      asm volatile("" : "+v"(wph));
#pragma unroll
      for (int s = 0; s < 16; s++) asm volatile("" : "+v"(aux[s]));
    }
    BAR();
    STAMP(13);
    rd(0, 0);
#pragma unroll
    for (int i = 0; i < 8; i++) {
      rd(i, 1);
      __builtin_amdgcn_sched_barrier(0);
      mm(0, 0, 6); extra(i, 0);
      mm(1, 0, 3); extra(i, 1);
      __builtin_amdgcn_sched_barrier(0);
      if (i < 7) {
        if (i < 5) WAITV(10); else if (i == 5) WAITV(9); else WAITV(8);
        LBAR();
        STAMP(14 + i);
        rd(i + 1, 0);
        if (i < 4) stage(i + 4);
        if (i == 4) { round_ld(0, 0); round_ld(1, 1); }
        if (i == 5) { round_ld(2, 2); round_ld(3, 3); }
        if (i == 6 && M) { round_ld(4, 4); round_ld(5, 5); round_ld(6, 6); }
        __builtin_amdgcn_sched_barrier(0);
      }
      mm(1, 3, 6);
    }
  };

  u64 tag;
  v4u phq[2][2];
  if (isM) {
    kloop(std::true_type{});
    STAMP(1);
    { u64 z = (((u64)ev[1] << 32) | ev[0]) + 0x9E3779B97F4A7C15ull; z = (z ^ (z >> 30)) * 0xBF58476D1CE4E5B9ull; z = (z ^ (z >> 27)) * 0x94D049BB133111EBull; tag = z ^ (z >> 31); }
    ssq = sum32(sum16(ssq));
    if (g == 0) {
      const float mean = ax[0] * (1.f / D);
      const float var = fmaxf(ssq * (1.f / D) - mean * mean, 0.f);
      *(LAS f32x2*)(lds + STAT + (w * 16 + j) * 8) = (f32x2){mean, __builtin_amdgcn_rsqf(var + 1e-5f)};
    }
    if (j == 0) {
      *(LAS f32x4*)(lds + STAT + 1024 + ((ch * 6 + tg) * 16 + 4 * g) * 4) = cs[0];
      *(LAS f32x4*)(lds + STAT + 1024 + ((ch * 6 + cs1) * 16 + 4 * g) * 4) = cs[1];
    }
    LBAR();
    round_ld(8, 8); round_ld(9, 9);
    f32x2 st[2];
#pragma unroll
    for (int a = 0; a < 2; a++) st[a] = *(LAS f32x2*)(lds + STAT + ((tg * 2 + a) * 16 + j) * 8);
#pragma unroll
    for (int sgm = 0; sgm < 3; sgm++) {
      f32x4 csv[2];
#pragma unroll
      for (int t2 = 0; t2 < 2; t2++) csv[t2] = *(LAS f32x4*)(lds + STAT + 1024 + ((ch * 6 + sgm * 2 + t2) * 16 + 4 * g) * 4);
      const int lu = 2 * sgm + ch;
#pragma unroll
      for (int a = 0; a < 2; a++) {
        f32x4 hv[2];
#pragma unroll
        for (int t2 = 0; t2 < 2; t2++)
#pragma unroll
          for (int r = 0; r < 4; r++) {
            const unsigned bb = b1r[sgm * 2 + t2][r >> 1];
            const float bias = (r & 1) ? bfhi(bb) : bflo(bb);
            const float v = __builtin_fmaf(__builtin_fmaf(-st[a][0], csv[t2][r], acc[a][sgm * 2 + t2][r]), st[a][1], bias);
            const float u = v * __builtin_fmaf(v * v, -0.044715f * 2.302208198f, -2.302208198f);
            hv[t2][r] = v * __builtin_amdgcn_rcpf(1.f + __builtin_amdgcn_exp2f(u));
          }
        if (lu < 5 || grp < 3)
          __builtin_amdgcn_raw_buffer_store_b128(pack8(hv[0], hv[1]), rs_ws, WS_ACT + (((rt * 4 + tg) * 60 + u0 + lu) * 2 + a) * 1024 + lane * 16, 0, AUX_SC1);
      }
    }
    STAMP(2);
    publish((GAS u64*)(wsp + WS_HF) + ((rt * 4 + tg) * 16 + grp * 2 + ch), tag, lane);
    STAMP(3);
  } else {
    kloop(std::false_type{});
    STAMP(1);
    { u64 z = (((u64)ev[1] << 32) | ev[0]) + 0x9E3779B97F4A7C15ull; z = (z ^ (z >> 30)) * 0xBF58476D1CE4E5B9ull; z = (z ^ (z >> 27)) * 0x94D049BB133111EBull; tag = z ^ (z >> 31); }
    LAS char* mine = lds + IMG + (2 * ch * 128 + 32 * tg) * 96;
    *(LAS v4u*)(mine + lane * 16) = wph;
    v4u wp[2];
    const bool act = (j >> 3) == (g >> 1);
#pragma unroll
    for (int m = 0; m < 2; m++) {
      wp[m] = trr2(mine + ((g >> 1) * 16 + 4 * (g & 1) + q4) * 32 + (8 * m + 4 * (p4 & 1)) * 2, 256);
#pragma unroll
      for (int e = 0; e < 4; e++) wp[m][e] = act ? wp[m][e] : 0u;
    }
    const f32x4 zero = {0.f, 0.f, 0.f, 0.f};
#pragma unroll
    for (int a = 0; a < 2; a++) {
      const v4u kf = pack8(acc[a][0], acc[a][1]);
      v4u pl, mi;
      dual_softmax(mfma(wp[0], kf, zero), mfma(wp[1], kf, zero), pl, mi);
      LAS char* row = lds + IMG + ((2 * ch + (g >> 1)) * 128 + 32 * tg + 16 * a + j) * 96;
      *(LAS v4u*)(row + 16 * (g & 1)) = pl;
      *(LAS v4u*)(row + 32 + 16 * (g & 1)) = mi;
      *(LAS v2u*)(row + 64 + 8 * (g & 1)) = (v2u){pk(acc[a][2][0], acc[a][2][1]), pk(acc[a][2][2], acc[a][2][3])};
      *(LAS v2u*)(row + 80 + 8 * (g & 1)) = (v2u){pk(acc[a][3][0], acc[a][3][1]), pk(acc[a][3][2], acc[a][3][3])};
    }
    LBAR();
    f32x4 kv = zero, zz = zero;
    LAS char* ib = lds + IMG + ((w >> 1) * 128 + 4 * g + q4) * 96 + 8 * p4;
#pragma unroll
    for (int ks = 0; ks < 4; ks++) {
      const v4u pa = trr2(ib + ks * 32 * 96 + 32 * (w & 1), 16 * 96), vb = trr2(ib + ks * 32 * 96 + 64, 16 * 96);
      kv = mfma(pa, vb, kv); zz = mfma(pa, ones, zz);
    }
    __builtin_amdgcn_raw_buffer_store_b128(__builtin_bit_cast(v4u, kv), rs_ws, WS_KVP + ((grp * 16 + rt) * 8 + w) * 1024 + lane * 16, 0, AUX_SC1);
    if (j == 0) __builtin_amdgcn_raw_buffer_store_b128(__builtin_bit_cast(v4u, zz), rs_ws, WS_ZP + ((grp * 8 + w) * 16 + rt) * 64 + g * 16, 0, AUX_SC1);
    STAMP(2);
    publish((GAS u64*)(wsp + WS_KVF) + ((grp * 8 + w) * 16 + rt), tag, lane);
    STAMP(3);
#pragma unroll
    for (int a = 0; a < 2; a++) {
      const v4u qf = pack8(acc[a][4], acc[a][5]);
      dual_softmax(mfma(wp[0], qf, zero), mfma(wp[1], qf, zero), phq[a][0], phq[a][1]);
    }
    if (g == 0 && grp == 0) {
      const float gate = __builtin_amdgcn_rcpf(1.f + __builtin_amdgcn_exp2f(-1.4426950408889634f * ax[0]));
      __builtin_amdgcn_raw_buffer_store_b32(__builtin_bit_cast(unsigned, gate), rs_ws, WS_G + (rt * 128 + w * 16 + j) * 4, 0, AUX_SC1);
    }
  }

  const int th = w >> 2, q = w & 3;
  int ab[4];
  { const int kk = 16 * (g >> 1) + 4 * (g & 1) + q4, u6 = kk & 6;
#pragma unroll
    for (int c = 0; c < 4; c++) ab[c] = q * 4096 + kk * 128 + (((2 * c) ^ u6) + (p4 >> 1)) * 16 + 8 * (p4 & 1); }
  BAR();
  v4u wt[4][4];
#pragma unroll
  for (int r = 0; r < 4; r++)
#pragma unroll
    for (int c = 0; c < 4; c++) wt[r][c] = trr2(lds + r * PAGE + ab[c], 1024);
  LBAR();
  if (isM) round_ld(7, 7);
  else { round_ld(4, 4); round_ld(5, 5); round_ld(6, 6); round_ld(7, 7); round_ld(8, 8); }
  round_ld(10, 0); round_ld(11, 1); round_ld(12, 2); round_ld(13, 3);
  STAMP(4);

  if (!isM) {
    wait16((const GAS u64*)(wsp + WS_KVF) + (grp * 8 + w) * 16, tag, lane);
    STAMP(5);
    v4u pt[16];
#pragma unroll
    for (int r = 0; r < 16; r++) pt[r] = __builtin_amdgcn_raw_buffer_load_b128(rs_ws, WS_KVP + (grp * 16 * 8 + w) * 1024 + lane * 16, r * 8192, AUX_SC1);
    const v4u zr = __builtin_amdgcn_raw_buffer_load_b128(rs_ws, WS_ZP + (grp * 8 + w) * 1024 + lane * 16, 0, AUX_SC1);
    __builtin_amdgcn_sched_barrier(0);
    f32x4 kv = __builtin_bit_cast(f32x4, pt[0]);
#pragma unroll
    for (int r = 1; r < 16; r++) kv += __builtin_bit_cast(f32x4, pt[r]);
    *(LAS v2u*)(lds + KVR + ((w >> 1) * 17 + j) * 64 + (16 * (w & 1) + 4 * g) * 2) = (v2u){pk(kv[0], kv[1]), pk(kv[2], kv[3])};
    f32x4 z = __builtin_bit_cast(f32x4, zr);
#pragma unroll
    for (int e = 0; e < 4; e++) {
      float v = z[e];
      v += __builtin_bit_cast(float, __builtin_amdgcn_update_dpp(0u, __builtin_bit_cast(unsigned, v), 0x124, 0xf, 0xf, false));
      v += __builtin_bit_cast(float, __builtin_amdgcn_update_dpp(0u, __builtin_bit_cast(unsigned, v), 0x128, 0xf, 0xf, false));
      z[e] = sum32(sum16(v));
    }
    if (lane < 4) *(LAS v2u*)(lds + KVR + ((w >> 1) * 17 + 16) * 64 + (16 * (w & 1) + 4 * lane) * 2) = (v2u){pk(z[0], z[1]), pk(z[2], z[3])};
    if (tid == 0) *(LAS v4u*)(lds + KVR_ZERO) = (v4u){0u, 0u, 0u, 0u};
    LBAR();
    STAMP(6);
    const bool act = (j >> 3) == (g >> 1);
    const int hb = KVR + (2 * ch + (g >> 1)) * 17 * 64 + 16 * (g & 1);
    v4u ka[2][2], za[2];
#pragma unroll
    for (int sg = 0; sg < 2; sg++) {
#pragma unroll
      for (int dh = 0; dh < 2; dh++) ka[sg][dh] = *(LAS v4u*)(lds + (act ? hb + (8 * dh + (j & 7)) * 64 + 32 * sg : KVR_ZERO));
      za[sg] = *(LAS v4u*)(lds + (act ? hb + 16 * 64 + 32 * sg : KVR_ZERO));
    }
    const f32x4 zero = {0.f, 0.f, 0.f, 0.f};
#pragma unroll
    for (int a = 0; a < 2; a++) {
      f32x4 n0 = mfma(ka[1][0], phq[a][1], mfma(ka[0][0], phq[a][0], zero));
      f32x4 n1 = mfma(ka[1][1], phq[a][1], mfma(ka[0][1], phq[a][0], zero));
      const f32x4 dn = mfma(za[1], phq[a][1], mfma(za[0], phq[a][0], zero));
      const float rd = 1.f / (dn[0] + 1e-6f);
      n0 *= rd; n1 *= rd;
      __builtin_amdgcn_raw_buffer_store_b128(pack8(n0, n1), rs_ws, WS_ACT + (((rt * 4 + tg) * 60 + 44 + 2 * grp + ch) * 2 + a) * 1024 + lane * 16, 0, AUX_SC1);
    }
    publish((GAS u64*)(wsp + WS_OF) + ((rt * 4 + tg) * 16 + grp * 2 + ch), tag, lane);
    STAMP(7);
    BAR();
    round_ld(9, 9);
  }

  const int tgi = panel * 2 + th;
  wait16((const GAS u64*)(wsp + WS_HF) + tgi * 16, tag, lane);
  STAMP(8);
  const unsigned av = WS_ACT + (tgi * 60 + q) * 2048 + lane * 16;
  v4u bf[11][2];
#pragma unroll
  for (int r = 0; r < 11; r++)
#pragma unroll
    for (int a = 0; a < 2; a++)
      bf[r][a] = __builtin_amdgcn_raw_buffer_load_b128(rs_ws, av + a * 1024, (r == 10 && q == 3) ? WS_END : r * 8192, AUX_SC1);
  v2u b2r[4];
#pragma unroll
  for (int c = 0; c < 4; c++) b2r[c] = *(const GAS v2u*)((const GAS char*)p.b2 + (ct * 64 + 16 * c + 4 * g) * 2);
  __builtin_amdgcn_sched_barrier(0);
  f32x4 ob[2][4], oa[2][4];
#pragma unroll
  for (int a = 0; a < 2; a++)
#pragma unroll
    for (int c = 0; c < 4; c++) { ob[a][c] = (f32x4){0.f, 0.f, 0.f, 0.f}; oa[a][c] = (f32x4){0.f, 0.f, 0.f, 0.f}; }
#pragma unroll
  for (int r = 0; r < 11; r++) {
    if (r == 7) BAR();
#pragma unroll
    for (int c = 0; c < 4; c++) {
      const v4u wf = r < 4 ? wt[r < 4 ? r : 0][c] : trr2(lds + (r < 10 ? r : 0) * PAGE + ab[c], 1024);
#pragma unroll
      for (int a = 0; a < 2; a++) ob[a][c] = mfma(wf, bf[r][a], ob[a][c]);
    }
  }
  STAMP(9);
  BAR();
  {
    const unsigned pv = (32 * q + (lane >> 3)) * 1024 + ct * 128 + (((lane & 7) ^ ((lane >> 3) & 6)) * 16);
#pragma unroll
    for (int i = 0; i < 4; i++) glds(rs_wo, lds + 4 * PAGE + w * 4096 + i * 1024, pv + i * 8192, 3 * 131072);
  }
  if (b == 0 && w == 7) {
#pragma nounroll
    for (unsigned spins = 0; spins < SPIN_CAP; ++spins) {
      bool ok = true;
#pragma unroll
      for (int i = 0; i < 16; i++) ok &= __hip_atomic_load((const GAS u64*)(wsp + WS_HF) + i * 64 + lane, RLX_AGENT) == tag;
      if (__all(ok)) break;
      __builtin_amdgcn_s_sleep(1);
    }
  }
  wait16((const GAS u64*)(wsp + WS_OF) + tgi * 16, tag, lane);
  STAMP(10);
  if (b == 0 && w == 7 && lane == 0) __hip_atomic_store((GAS u64*)(wsp + WS_E), tag, RLX_AGENT);
  float gate[2];
  v4u of[4][2];
#pragma unroll
  for (int r = 0; r < 4; r++)
#pragma unroll
    for (int a = 0; a < 2; a++) of[r][a] = __builtin_amdgcn_raw_buffer_load_b128(rs_ws, av + a * 1024, (11 + r) * 8192, AUX_SC1);
#pragma unroll
  for (int a = 0; a < 2; a++) gate[a] = __builtin_bit_cast(float, __builtin_amdgcn_raw_buffer_load_b32(rs_ws, WS_G + (tgi * 32 + 16 * a + j) * 4, 0, AUX_SC1));
  __builtin_amdgcn_sched_barrier(0);
#pragma unroll
  for (int r = 0; r < 4; r++)
#pragma unroll
    for (int c = 0; c < 4; c++) {
      const v4u wf = trr2(lds + (r < 3 ? (r + 1) * PAGE + ab[c] : 4 * PAGE + (w - q) * 4096 + ab[c]), 1024);
#pragma unroll
      for (int a = 0; a < 2; a++) oa[a][c] = mfma(wf, of[r][a], oa[a][c]);
    }
  STAMP(11);
#pragma unroll
  for (int a = 0; a < 2; a++)
#pragma unroll
    for (int c = 0; c < 4; c++) {
      f32x4 v;
#pragma unroll
      for (int r = 0; r < 4; r++) {
        const unsigned bb = b2r[c][r >> 1];
        const float bias = q == 0 ? ((r & 1) ? bfhi(bb) : bflo(bb)) : 0.f;
        v[r] = gate[a] * oa[a][c][r] + (1.f - gate[a]) * (ob[a][c][r] + bias);
      }
      *(LAS f32x4*)(lds + RED + (q * 64 + 32 * th + 16 * a + j) * 256 + (((4 * c + g) ^ (j & 7)) * 16)) = v;
    }
  LBAR();
  {
    const int tok = tid >> 3, c8 = tid & 7;
    f32x4 s0 = {0.f, 0.f, 0.f, 0.f}, s1 = {0.f, 0.f, 0.f, 0.f};
#pragma unroll
    for (int qq = 0; qq < 4; qq++) {
      s0 += *(LAS f32x4*)(lds + RED + (qq * 64 + tok) * 256 + (((2 * c8) ^ (tok & 7)) * 16));
      s1 += *(LAS f32x4*)(lds + RED + (qq * 64 + tok) * 256 + (((2 * c8 + 1) ^ (tok & 7)) * 16));
    }
    *(GAS v4u*)((GAS char*)p.out + ((panel * 64 + tok) * 512 + ct * 64 + c8 * 8) * 2) = pack8(s0, s1);
  }
  STAMP(12);
}
}

extern "C" void kernel_launch(void* const* d_in, const int* in_sizes, int n_in, void* d_out, int out_size, void* d_ws, size_t ws_size,
                              hipStream_t stream) {
  gm::Params p;
  p.x = d_in[0]; p.wqkv = d_in[1]; p.wphi = d_in[2]; p.wo = d_in[3];
  p.w1 = d_in[5]; p.b1 = d_in[6]; p.w2 = d_in[7]; p.b2 = d_in[8]; p.gw = d_in[9];
  p.out = d_out; p.ws = d_ws;
  gm::fused<<<256, 512, 0, stream>>>(p);
}
```

```cpp
#include <hip/hip_runtime.h>
#include <hip/hip_bf16.h>

typedef unsigned long long u64;
typedef unsigned v4u __attribute__((ext_vector_type(4)));
typedef unsigned v2u __attribute__((ext_vector_type(2)));
typedef float f32x4 __attribute__((ext_vector_type(4)));
typedef float f32x2 __attribute__((ext_vector_type(2)));
typedef __bf16 bf16x8 __attribute__((ext_vector_type(8)));
typedef __bf16 bf16x2 __attribute__((ext_vector_type(2)));
typedef short s16x4 __attribute__((ext_vector_type(4)));
#define GAS __attribute__((address_space(1)))
#define LAS __attribute__((address_space(3)))
#define DI __device__ __forceinline__
#define RLX_AGENT __ATOMIC_RELAXED, __HIP_MEMORY_SCOPE_AGENT

#define STAMPS 0

namespace gm {
constexpr int S = 2048, D = 512, F = 1376;
constexpr unsigned WS_E = 0, WS_KVF = 4096, WS_HF = 12288, WS_OF = 20480, WS_G = 32768, WS_ZP = 40960, WS_KVP = 131072,
                   WS_ACT = 2u << 20, ACT_BYTES = 16u * 4 * 60 * 2048, WS_END = WS_ACT + ACT_BYTES, WS_STAMP = 16u << 20;
constexpr int SLOT = 40960, WOFF = 16384, PAGE = 16384;
constexpr int IMG = 73728;
constexpr int KVR = 9 * PAGE;
constexpr int KVR_ZERO = KVR + 4 * 17 * 64;
constexpr int STAT = 7 * PAGE;
constexpr int RED = 6 * PAGE;
constexpr int AUX_SC1 = 16;
constexpr unsigned SPIN_CAP = 1u << 22;

struct Params {
  const void *x, *wqkv, *wphi, *wo, *w1, *b1, *w2, *b2, *gw;
  void *out, *ws;
};

DI float asf(unsigned u) { return __builtin_bit_cast(float, u); }
DI unsigned asu(float f) { return __builtin_bit_cast(unsigned, f); }
DI bf16x2 asb2(unsigned u) { return __builtin_bit_cast(bf16x2, u); }
DI f32x4 mfma(v4u a, v4u b, f32x4 c) {
  return __builtin_amdgcn_mfma_f32_16x16x32_bf16(__builtin_bit_cast(bf16x8, a), __builtin_bit_cast(bf16x8, b), c, 0, 0, 0);
}
DI unsigned pk(float a, float b) { return __builtin_bit_cast(unsigned, __builtin_convertvector((f32x2){a, b}, bf16x2)); }
DI float bflo(unsigned u) { return __builtin_bit_cast(float, u << 16); }
DI float bfhi(unsigned u) { return __builtin_bit_cast(float, u & 0xffff0000u); }
DI v2u trr(LAS char* p) { return __builtin_bit_cast(v2u, __builtin_amdgcn_ds_read_tr16_b64_v4i16((LAS s16x4*)p)); }
DI v4u trr2(LAS char* p, int off2) { v2u a = trr(p), b = trr(p + off2); return (v4u){a[0], a[1], b[0], b[1]}; }
DI f32x2 pair16(float v) { auto r = __builtin_amdgcn_permlane16_swap(asu(v), asu(v), false, false); return (f32x2){asf(r[0]), asf(r[1])}; }
DI f32x2 pair32(float v) { auto r = __builtin_amdgcn_permlane32_swap(asu(v), asu(v), false, false); return (f32x2){asf(r[0]), asf(r[1])}; }
DI float sum16(float v) { const f32x2 r = pair16(v); return r[0] + r[1]; }
DI float sum32(float v) { const f32x2 r = pair32(v); return r[0] + r[1]; }
DI float max16(float v) { const f32x2 r = pair16(v); return fmaxf(r[0], r[1]); }
DI float min16(float v) { const f32x2 r = pair16(v); return fminf(r[0], r[1]); }
#define WAITV(n) asm volatile("s_waitcnt vmcnt(%0)" ::"n"(n) : "memory")
#define WAITL() asm volatile("s_waitcnt lgkmcnt(0)" ::: "memory")
#define BAR() do { asm volatile("" ::: "memory"); __builtin_amdgcn_s_barrier(); asm volatile("" ::: "memory"); } while (0)
#define LBAR() do { WAITL(); __builtin_amdgcn_s_barrier(); asm volatile("" ::: "memory"); } while (0)

DI void glds(__amdgpu_buffer_rsrc_t rs, LAS char* dst, unsigned voff, unsigned soff) {
  asm volatile("s_mov_b32 m0, %0\n\ts_nop 0\n\tbuffer_load_dwordx4 %1, %2, %3 offen lds" ::"s"((unsigned)(size_t)dst), "v"(voff), "s"(rs), "s"(soff) : "memory");
}
DI void dual_softmax(const f32x4& p0, const f32x4& p1, v4u& plus, v4u& minus) {
  float mx = fmaxf(fmaxf(fmaxf(p0[0], p0[1]), fmaxf(p0[2], p0[3])), fmaxf(fmaxf(p1[0], p1[1]), fmaxf(p1[2], p1[3])));
  float mn = fminf(fminf(fminf(p0[0], p0[1]), fminf(p0[2], p0[3])), fminf(fminf(p1[0], p1[1]), fminf(p1[2], p1[3])));
  mx = max16(mx); mn = min16(mn);
  constexpr float L2E = 1.4426950408889634f;
  const float mxs = -mx * L2E, mns = mn * L2E;
  float ep[8], em[8], sp = 0.f, sm = 0.f;
#pragma unroll
  for (int e = 0; e < 8; e++) {
    const float p = e < 4 ? p0[e & 3] : p1[e & 3];
    ep[e] = __builtin_amdgcn_exp2f(__builtin_fmaf(p, L2E, mxs));
    em[e] = __builtin_amdgcn_exp2f(__builtin_fmaf(p, -L2E, mns));
    sp += ep[e]; sm += em[e];
  }
  sp = __builtin_amdgcn_rcpf(sum16(sp)); sm = __builtin_amdgcn_rcpf(sum16(sm));
#pragma unroll
  for (int e = 0; e < 4; e++) { plus[e] = pk(ep[2 * e] * sp, ep[2 * e + 1] * sp); minus[e] = pk(em[2 * e] * sm, em[2 * e + 1] * sm); }
}
DI v4u pack8(const f32x4& a, const f32x4& b) { return (v4u){pk(a[0], a[1]), pk(a[2], a[3]), pk(b[0], b[1]), pk(b[2], b[3])}; }
DI void wait16(const GAS u64* line, u64 tag, int lane) {
#pragma nounroll
  for (unsigned spins = 0; spins < SPIN_CAP; ++spins) {
    const u64 v = __hip_atomic_load(line + (lane & 15), RLX_AGENT);
    if (__all(v == tag)) break;
    __builtin_amdgcn_s_sleep(1);
  }
  __builtin_amdgcn_fence(__ATOMIC_ACQUIRE, "wavefront");
}
DI void publish(GAS u64* flag, u64 tag, int lane) {
  WAITV(0);
  if (lane == 0) __hip_atomic_store(flag, tag, RLX_AGENT);
}

#define STAMP(i) do {} while (0)

__global__ void __launch_bounds__(512, 2) fused(Params p) {
  __shared__ __attribute__((aligned(16))) char smem[163840];
  LAS char* lds = (LAS char*)smem;
  const int tid = threadIdx.x, lane = tid & 63, w = __builtin_amdgcn_readfirstlane(tid >> 6);
  const int g = lane >> 4, j = lane & 15, q4 = (lane & 15) >> 2, p4 = lane & 3;
  const int tg = w >> 1, ch = w & 1;
  const int b = blockIdx.x, xcd = b & 7, idx = b >> 3;
  const int rt = (xcd >> 2) * 8 + (idx >> 2), c4 = idx & 3;
  const bool isM = c4 >= 2;
  const int grp = (xcd & 3) * 2 + (c4 & 1);
  const int u0 = grp < 3 ? 6 * grp : 18 + 5 * (grp - 3);
  const int rot = idx >> 2;
  const int panel = xcd * 4 + (idx >> 3), ct = idx & 7;
  STAMP(0);

  const __amdgpu_buffer_rsrc_t rs_ws = __builtin_amdgcn_make_buffer_rsrc(p.ws, 0, WS_END, 0x00020000);
  const __amdgpu_buffer_rsrc_t rs_x = __builtin_amdgcn_make_buffer_rsrc((void*)p.x, 0, S * D * 2, 0x00020000);
  const __amdgpu_buffer_rsrc_t rs_w = __builtin_amdgcn_make_buffer_rsrc((void*)(isM ? p.w1 : p.wqkv), 0, isM ? D * F * 2 : D * 1536 * 2, 0x00020000);
  const __amdgpu_buffer_rsrc_t rs_w2 = __builtin_amdgcn_make_buffer_rsrc((void*)p.w2, 0, F * D * 2, 0x00020000);
  const __amdgpu_buffer_rsrc_t rs_wo = __builtin_amdgcn_make_buffer_rsrc((void*)p.wo, 0, D * D * 2, 0x00020000);
  GAS char* wsp = (GAS char*)p.ws;

  {
    unsigned long long pc;
    asm volatile("s_getpc_b64 %0" : "=s"(pc));
    pc &= ~255ull;
    if (w < 2) {
      const GAS char* a = (const GAS char*)pc + (w * 32 + idx) * 1024 + lane * 16;
      if (w * 32 + idx < 32) asm volatile("s_mov_b32 m0, %0\n\ts_nop 0\n\tglobal_load_lds_dwordx4 %1, off" ::"s"(2 * w * 1024), "v"(a) : "memory");
    }

  }
  v2u ev;
  asm volatile("global_load_dwordx2 %0, %1, off sc1" : "=v"(ev) : "v"(wsp + WS_E) : "memory");
  v4u aux[16]; v2u b1r[6]; v4u wph;

  unsigned xv[2], wv[3];
#pragma unroll
  for (int i = 0; i < 2; i++) { const int row = 8 * (2 * w + i) + (lane >> 3); xv[i] = (rt * 128 + row) * 1024 + (((lane & 7) ^ ((row >> 1) & 7)) * 16); }
#pragma unroll
  for (int i = 0; i < 3; i++) {
    const int n = (3 * w + i) * 64 + lane, k = n / 24, cp = n % 24;
    const int c = cp ^ (((k >> 1) & 1) | (((k >> 3) & 1) << 2));
    const int seg = c >> 3;
    wv[i] = isM ? k * 2752 + u0 * 64 + c * 16 : k * 3072 + (seg == 2 ? 0 : (seg + 1) * 1024) + grp * 128 + (c & 7) * 16;
  }
  const unsigned wstep = isM ? 64 * 2752 : 64 * 3072;
  const unsigned w2v = (16 * w + (lane >> 3)) * 1024 + ct * 128 + (((lane & 7) ^ ((lane >> 3) & 6)) * 16);
  auto stage = [&](int t) {
    const int kt = (t + rot) & 7;
    LAS char* dst = lds + (t & 3) * SLOT;
    glds(rs_x, dst + (2 * w) * 1024, xv[0], kt * 128);
    glds(rs_x, dst + (2 * w + 1) * 1024, xv[1], kt * 128);
#pragma unroll
    for (int i = 0; i < 3; i++) glds(rs_w, dst + WOFF + (3 * w + i) * 1024, wv[i], kt * wstep);
  };
  auto round_ld = [&](int r, int page) {
    LAS char* dst = lds + page * PAGE + w * 2048;
    if (r < 11) { glds(rs_w2, dst, w2v, r * 131072); glds(rs_w2, dst + 1024, w2v + 8192, r * 131072); }
    else { glds(rs_wo, dst, w2v, (r - 11) * 131072); glds(rs_wo, dst + 1024, w2v + 8192, (r - 11) * 131072); }
  };

  const int xb0 = (32 * tg + j) * 128 + ((g ^ (j >> 1)) * 16);
  const int sk = ((q4 >> 1) & 1) | ((g & 1) << 2);
  const int wrow = WOFF + (8 * g + q4) * 384 + 8 * (p4 & 1);
  const int wb0 = wrow + 16 * ((4 * ch + 2 * (p4 >> 1)) ^ sk), wb1 = wrow + 16 * ((4 * ch + 2 * (p4 >> 1) + 1) ^ sk);
  const int cs1 = tg < 2 ? tg + 4 : tg;
  const int cb0 = ((tg & 1) ? wb1 : wb0) + (tg >> 1) * 128, cb1 = ((cs1 & 1) ? wb1 : wb0) + (cs1 >> 1) * 128;

  f32x4 acc[2][6];
#pragma unroll
  for (int a = 0; a < 2; a++)
#pragma unroll
    for (int t = 0; t < 6; t++) acc[a][t] = (f32x4){0.f, 0.f, 0.f, 0.f};
  f32x4 cs[2] = {{0.f, 0.f, 0.f, 0.f}, {0.f, 0.f, 0.f, 0.f}}, ax = {0.f, 0.f, 0.f, 0.f};
  float ssq = 0.f;
  const v4u ones = {0x3F803F80u, 0x3F803F80u, 0x3F803F80u, 0x3F803F80u};

  auto kloop = [&](auto ism) {
    constexpr bool M = decltype(ism)::value;
    v4u xf[2][2], wf[2][6], cf[2][2];
    if (M) {
      const __amdgpu_buffer_rsrc_t rs_b1 = __builtin_amdgcn_make_buffer_rsrc((void*)p.b1, 0, F * 2, 0x00020000);
#pragma unroll
      for (int t = 0; t < 6; t++) {
        const unsigned off = ((u0 + 2 * (t >> 1) + ch) * 32 + 16 * (g >> 1) + 8 * (t & 1) + 4 * (g & 1)) * 2;
        asm volatile("buffer_load_dwordx2 %0, %1, %2, 0 offen" : "=v"(b1r[t]) : "v"(off), "s"(rs_b1) : "memory");
      }
    } else {
      const GAS char* a = (const GAS char*)p.wphi + (4 * grp + 2 * ch) * 512 + lane * 16;
      asm volatile("global_load_dwordx4 %0, %1, off" : "=v"(wph) : "v"(a) : "memory");
      const __amdgpu_buffer_rsrc_t rs_gw = __builtin_amdgcn_make_buffer_rsrc((void*)p.gw, 0, D * 2, 0x00020000);
#pragma unroll
      for (int s = 0; s < 16; s++)
        asm volatile("buffer_load_dwordx4 %0, %1, %2, %3 offen" : "=v"(aux[s]) : "v"(16 * g), "s"(rs_gw), "s"(((((s >> 1) + rot) & 7) * 64 + (s & 1) * 32) * 2) : "memory");
    }
    stage(0); stage(1); stage(2); stage(3);
    auto rd = [&](int t, int ks) {
      LAS char* sl = lds + (t & 3) * SLOT;
#pragma unroll
      for (int a = 0; a < 2; a++) xf[ks][a] = *(LAS v4u*)(sl + (xb0 ^ (ks * 64)) + a * 2048);
#pragma unroll
      for (int t6 = 0; t6 < 6; t6++) wf[ks][t6] = trr2(sl + ((t6 & 1) ? wb1 : wb0) + (t6 >> 1) * 128 + ks * 12288, 1536);
      if (M) { cf[ks][0] = trr2(sl + cb0 + ks * 12288, 1536); cf[ks][1] = trr2(sl + cb1 + ks * 12288, 1536); }
    };
    auto mm = [&](int ks, int t0, int t1) {
#pragma unroll
      for (int t6 = t0; t6 < t1; t6++)
#pragma unroll
        for (int a = 0; a < 2; a++) acc[a][t6] = mfma(wf[ks][t6], xf[ks][a], acc[a][t6]);
    };
    auto extra = [&](int i, int ks) {
      const v4u xs = ch ? xf[ks][1] : xf[ks][0];
      if (M) {
        cs[0] = mfma(cf[ks][0], ones, cs[0]); cs[1] = mfma(cf[ks][1], ones, cs[1]);
        ax = mfma(ones, xs, ax);
#pragma unroll
        for (int e = 0; e < 4; e++) ssq = __builtin_amdgcn_fdot2_f32_bf16(asb2(xs[e]), asb2(xs[e]), ssq, false);
        asm volatile("" : "+v"(cs[0]), "+v"(cs[1]), "+v"(ssq));
      } else ax = mfma(aux[2 * i + ks], xs, ax);
      asm volatile("" : "+v"(ax));
    };
    STAMP(21);
    WAITV(15);
    asm volatile("" : "+v"(ev));
    if (M) {
#pragma unroll
      for (int t = 0; t < 6; t++) asm volatile("" : "+v"(b1r[t]));
    } else {
      asm volatile("" : "+v"(wph));
#pragma unroll
      for (int s = 0; s < 16; s++) asm volatile("" : "+v"(aux[s]));
    }
    BAR();
    STAMP(13);
    rd(0, 0);
#pragma unroll
    for (int i = 0; i < 8; i++) {
      rd(i, 1);
      __builtin_amdgcn_sched_barrier(0);
      mm(0, 0, 6); extra(i, 0);
      mm(1, 0, 3); extra(i, 1);
      __builtin_amdgcn_sched_barrier(0);
      if (i < 7) {
        if (i < 5) WAITV(10); else if (i == 5) WAITV(9); else WAITV(8);
        LBAR();
        STAMP(14 + i);
        rd(i + 1, 0);
        if (i < 4) stage(i + 4);
        if (i == 4) { round_ld(0, 0); round_ld(1, 1); }
        if (i == 5) { round_ld(2, 2); round_ld(3, 3); }
        if (i == 6 && M) { round_ld(4, 4); round_ld(5, 5); round_ld(6, 6); }
        __builtin_amdgcn_sched_barrier(0);
      }
      mm(1, 3, 6);
    }
  };

  u64 tag;
  v4u phq[2][2];
  if (isM) {
    kloop(std::true_type{});
    STAMP(1);
    { u64 z = (((u64)ev[1] << 32) | ev[0]) + 0x9E3779B97F4A7C15ull; z = (z ^ (z >> 30)) * 0xBF58476D1CE4E5B9ull; z = (z ^ (z >> 27)) * 0x94D049BB133111EBull; tag = z ^ (z >> 31); }
    ssq = sum32(sum16(ssq));
    if (g == 0) {
      const float mean = ax[0] * (1.f / D);
      const float var = fmaxf(ssq * (1.f / D) - mean * mean, 0.f);
      *(LAS f32x2*)(lds + STAT + (w * 16 + j) * 8) = (f32x2){mean, __builtin_amdgcn_rsqf(var + 1e-5f)};
    }
    if (j == 0) {
      *(LAS f32x4*)(lds + STAT + 1024 + ((ch * 6 + tg) * 16 + 4 * g) * 4) = cs[0];
      *(LAS f32x4*)(lds + STAT + 1024 + ((ch * 6 + cs1) * 16 + 4 * g) * 4) = cs[1];
    }
    LBAR();
    round_ld(8, 8); round_ld(9, 9);
    f32x2 st[2];
#pragma unroll
    for (int a = 0; a < 2; a++) st[a] = *(LAS f32x2*)(lds + STAT + ((tg * 2 + a) * 16 + j) * 8);
#pragma unroll
    for (int sgm = 0; sgm < 3; sgm++) {
      f32x4 csv[2];
#pragma unroll
      for (int t2 = 0; t2 < 2; t2++) csv[t2] = *(LAS f32x4*)(lds + STAT + 1024 + ((ch * 6 + sgm * 2 + t2) * 16 + 4 * g) * 4);
      const int lu = 2 * sgm + ch;
#pragma unroll
      for (int a = 0; a < 2; a++) {
        f32x4 hv[2];
#pragma unroll
        for (int t2 = 0; t2 < 2; t2++)
#pragma unroll
          for (int r = 0; r < 4; r++) {
            const unsigned bb = b1r[sgm * 2 + t2][r >> 1];
            const float bias = (r & 1) ? bfhi(bb) : bflo(bb);
            const float v = __builtin_fmaf(__builtin_fmaf(-st[a][0], csv[t2][r], acc[a][sgm * 2 + t2][r]), st[a][1], bias);
            const float u = v * __builtin_fmaf(v * v, -0.044715f * 2.302208198f, -2.302208198f);
            hv[t2][r] = v * __builtin_amdgcn_rcpf(1.f + __builtin_amdgcn_exp2f(u));
          }
        if (lu < 5 || grp < 3)
          __builtin_amdgcn_raw_buffer_store_b128(pack8(hv[0], hv[1]), rs_ws, WS_ACT + (((rt * 4 + tg) * 60 + u0 + lu) * 2 + a) * 1024 + lane * 16, 0, AUX_SC1);
      }
    }
    STAMP(2);
    publish((GAS u64*)(wsp + WS_HF) + ((rt * 4 + tg) * 16 + grp * 2 + ch), tag, lane);
    STAMP(3);
  } else {
    kloop(std::false_type{});
    STAMP(1);
    { u64 z = (((u64)ev[1] << 32) | ev[0]) + 0x9E3779B97F4A7C15ull; z = (z ^ (z >> 30)) * 0xBF58476D1CE4E5B9ull; z = (z ^ (z >> 27)) * 0x94D049BB133111EBull; tag = z ^ (z >> 31); }
    LAS char* mine = lds + IMG + (2 * ch * 128 + 32 * tg) * 96;
    *(LAS v4u*)(mine + lane * 16) = wph;
    v4u wp[2];
    const bool act = (j >> 3) == (g >> 1);
#pragma unroll
    for (int m = 0; m < 2; m++) {
      wp[m] = trr2(mine + ((g >> 1) * 16 + 4 * (g & 1) + q4) * 32 + (8 * m + 4 * (p4 & 1)) * 2, 256);
#pragma unroll
      for (int e = 0; e < 4; e++) wp[m][e] = act ? wp[m][e] : 0u;
    }
    const f32x4 zero = {0.f, 0.f, 0.f, 0.f};
#pragma unroll
    for (int a = 0; a < 2; a++) {
      const v4u kf = pack8(acc[a][0], acc[a][1]);
      v4u pl, mi;
      dual_softmax(mfma(wp[0], kf, zero), mfma(wp[1], kf, zero), pl, mi);
      LAS char* row = lds + IMG + ((2 * ch + (g >> 1)) * 128 + 32 * tg + 16 * a + j) * 96;
      *(LAS v4u*)(row + 16 * (g & 1)) = pl;
      *(LAS v4u*)(row + 32 + 16 * (g & 1)) = mi;
      *(LAS v2u*)(row + 64 + 8 * (g & 1)) = (v2u){pk(acc[a][2][0], acc[a][2][1]), pk(acc[a][2][2], acc[a][2][3])};
      *(LAS v2u*)(row + 80 + 8 * (g & 1)) = (v2u){pk(acc[a][3][0], acc[a][3][1]), pk(acc[a][3][2], acc[a][3][3])};
    }
    LBAR();
    f32x4 kv = zero, zz = zero;
    LAS char* ib = lds + IMG + ((w >> 1) * 128 + 4 * g + q4) * 96 + 8 * p4;
#pragma unroll
    for (int ks = 0; ks < 4; ks++) {
      const v4u pa = trr2(ib + ks * 32 * 96 + 32 * (w & 1), 16 * 96), vb = trr2(ib + ks * 32 * 96 + 64, 16 * 96);
      kv = mfma(pa, vb, kv); zz = mfma(pa, ones, zz);
    }
    __builtin_amdgcn_raw_buffer_store_b128(__builtin_bit_cast(v4u, kv), rs_ws, WS_KVP + ((grp * 16 + rt) * 8 + w) * 1024 + lane * 16, 0, AUX_SC1);
    if (j == 0) __builtin_amdgcn_raw_buffer_store_b128(__builtin_bit_cast(v4u, zz), rs_ws, WS_ZP + ((grp * 8 + w) * 16 + rt) * 64 + g * 16, 0, AUX_SC1);
    STAMP(2);
    publish((GAS u64*)(wsp + WS_KVF) + ((grp * 8 + w) * 16 + rt), tag, lane);
    STAMP(3);
#pragma unroll
    for (int a = 0; a < 2; a++) {
      const v4u qf = pack8(acc[a][4], acc[a][5]);
      dual_softmax(mfma(wp[0], qf, zero), mfma(wp[1], qf, zero), phq[a][0], phq[a][1]);
    }
    if (g == 0 && grp == 0) {
      const float gate = __builtin_amdgcn_rcpf(1.f + __builtin_amdgcn_exp2f(-1.4426950408889634f * ax[0]));
      __builtin_amdgcn_raw_buffer_store_b32(__builtin_bit_cast(unsigned, gate), rs_ws, WS_G + (rt * 128 + w * 16 + j) * 4, 0, AUX_SC1);
    }
  }

  const int th = w >> 2, q = w & 3;
  int ab[4];
  { const int kk = 16 * (g >> 1) + 4 * (g & 1) + q4, u6 = kk & 6;
#pragma unroll
    for (int c = 0; c < 4; c++) ab[c] = q * 4096 + kk * 128 + (((2 * c) ^ u6) + (p4 >> 1)) * 16 + 8 * (p4 & 1); }
  BAR();
  v4u wt[4][4];
#pragma unroll
  for (int r = 0; r < 4; r++)
#pragma unroll
    for (int c = 0; c < 4; c++) wt[r][c] = trr2(lds + r * PAGE + ab[c], 1024);
  LBAR();
  if (isM) round_ld(7, 7);
  else { round_ld(4, 4); round_ld(5, 5); round_ld(6, 6); round_ld(7, 7); round_ld(8, 8); }
  round_ld(10, 0); round_ld(11, 1); round_ld(12, 2); round_ld(13, 3);
  STAMP(4);

  if (!isM) {
    wait16((const GAS u64*)(wsp + WS_KVF) + (grp * 8 + w) * 16, tag, lane);
    STAMP(5);
    v4u pt[16];
#pragma unroll
    for (int r = 0; r < 16; r++) pt[r] = __builtin_amdgcn_raw_buffer_load_b128(rs_ws, WS_KVP + (grp * 16 * 8 + w) * 1024 + lane * 16, r * 8192, AUX_SC1);
    const v4u zr = __builtin_amdgcn_raw_buffer_load_b128(rs_ws, WS_ZP + (grp * 8 + w) * 1024 + lane * 16, 0, AUX_SC1);
    __builtin_amdgcn_sched_barrier(0);
    f32x4 kv = __builtin_bit_cast(f32x4, pt[0]);
#pragma unroll
    for (int r = 1; r < 16; r++) kv += __builtin_bit_cast(f32x4, pt[r]);
    *(LAS v2u*)(lds + KVR + ((w >> 1) * 17 + j) * 64 + (16 * (w & 1) + 4 * g) * 2) = (v2u){pk(kv[0], kv[1]), pk(kv[2], kv[3])};
    f32x4 z = __builtin_bit_cast(f32x4, zr);
#pragma unroll
    for (int e = 0; e < 4; e++) {
      float v = z[e];
      v += __builtin_bit_cast(float, __builtin_amdgcn_update_dpp(0u, __builtin_bit_cast(unsigned, v), 0x124, 0xf, 0xf, false));
      v += __builtin_bit_cast(float, __builtin_amdgcn_update_dpp(0u, __builtin_bit_cast(unsigned, v), 0x128, 0xf, 0xf, false));
      z[e] = sum32(sum16(v));
    }
    if (lane < 4) *(LAS v2u*)(lds + KVR + ((w >> 1) * 17 + 16) * 64 + (16 * (w & 1) + 4 * lane) * 2) = (v2u){pk(z[0], z[1]), pk(z[2], z[3])};
    if (tid == 0) *(LAS v4u*)(lds + KVR_ZERO) = (v4u){0u, 0u, 0u, 0u};
    LBAR();
    STAMP(6);
    const bool act = (j >> 3) == (g >> 1);
    const int hb = KVR + (2 * ch + (g >> 1)) * 17 * 64 + 16 * (g & 1);
    v4u ka[2][2], za[2];
#pragma unroll
    for (int sg = 0; sg < 2; sg++) {
#pragma unroll
      for (int dh = 0; dh < 2; dh++) ka[sg][dh] = *(LAS v4u*)(lds + (act ? hb + (8 * dh + (j & 7)) * 64 + 32 * sg : KVR_ZERO));
      za[sg] = *(LAS v4u*)(lds + (act ? hb + 16 * 64 + 32 * sg : KVR_ZERO));
    }
    const f32x4 zero = {0.f, 0.f, 0.f, 0.f};
#pragma unroll
    for (int a = 0; a < 2; a++) {
      f32x4 n0 = mfma(ka[1][0], phq[a][1], mfma(ka[0][0], phq[a][0], zero));
      f32x4 n1 = mfma(ka[1][1], phq[a][1], mfma(ka[0][1], phq[a][0], zero));
      const f32x4 dn = mfma(za[1], phq[a][1], mfma(za[0], phq[a][0], zero));
      const float rd = 1.f / (dn[0] + 1e-6f);
      n0 *= rd; n1 *= rd;
      __builtin_amdgcn_raw_buffer_store_b128(pack8(n0, n1), rs_ws, WS_ACT + (((rt * 4 + tg) * 60 + 44 + 2 * grp + ch) * 2 + a) * 1024 + lane * 16, 0, AUX_SC1);
    }
    publish((GAS u64*)(wsp + WS_OF) + ((rt * 4 + tg) * 16 + grp * 2 + ch), tag, lane);
    STAMP(7);
    BAR();
    round_ld(9, 9);
  }

  const int tgi = panel * 2 + th;
  wait16((const GAS u64*)(wsp + WS_HF) + tgi * 16, tag, lane);
  STAMP(8);
  const unsigned av = WS_ACT + (tgi * 60 + q) * 2048 + lane * 16;
  v4u bf[11][2];
#pragma unroll
  for (int r = 0; r < 11; r++)
#pragma unroll
    for (int a = 0; a < 2; a++)
      bf[r][a] = __builtin_amdgcn_raw_buffer_load_b128(rs_ws, av + a * 1024, (r == 10 && q == 3) ? WS_END : r * 8192, AUX_SC1);
  v2u b2r[4];
#pragma unroll
  for (int c = 0; c < 4; c++) b2r[c] = *(const GAS v2u*)((const GAS char*)p.b2 + (ct * 64 + 16 * c + 4 * g) * 2);
  __builtin_amdgcn_sched_barrier(0);
  f32x4 ob[2][4], oa[2][4];
#pragma unroll
  for (int a = 0; a < 2; a++)
#pragma unroll
    for (int c = 0; c < 4; c++) { ob[a][c] = (f32x4){0.f, 0.f, 0.f, 0.f}; oa[a][c] = (f32x4){0.f, 0.f, 0.f, 0.f}; }
#pragma unroll
  for (int r = 0; r < 11; r++) {
    if (r == 7) BAR();
#pragma unroll
    for (int c = 0; c < 4; c++) {
      const v4u wf = r < 4 ? wt[r < 4 ? r : 0][c] : trr2(lds + (r < 10 ? r : 0) * PAGE + ab[c], 1024);
#pragma unroll
      for (int a = 0; a < 2; a++) ob[a][c] = mfma(wf, bf[r][a], ob[a][c]);
    }
  }
  STAMP(9);
  BAR();
  {
    const unsigned pv = (32 * q + (lane >> 3)) * 1024 + ct * 128 + (((lane & 7) ^ ((lane >> 3) & 6)) * 16);
#pragma unroll
    for (int i = 0; i < 4; i++) glds(rs_wo, lds + 4 * PAGE + w * 4096 + i * 1024, pv + i * 8192, 3 * 131072);
  }
  if (b == 0 && w == 7) {
#pragma nounroll
    for (unsigned spins = 0; spins < SPIN_CAP; ++spins) {
      bool ok = true;
#pragma unroll
      for (int i = 0; i < 16; i++) ok &= __hip_atomic_load((const GAS u64*)(wsp + WS_HF) + i * 64 + lane, RLX_AGENT) == tag;
      if (__all(ok)) break;
      __builtin_amdgcn_s_sleep(1);
    }
  }
  wait16((const GAS u64*)(wsp + WS_OF) + tgi * 16, tag, lane);
  STAMP(10);
  if (b == 0 && w == 7 && lane == 0) __hip_atomic_store((GAS u64*)(wsp + WS_E), tag, RLX_AGENT);
  float gate[2];
  v4u of[4][2];
#pragma unroll
  for (int r = 0; r < 4; r++)
#pragma unroll
    for (int a = 0; a < 2; a++) of[r][a] = __builtin_amdgcn_raw_buffer_load_b128(rs_ws, av + a * 1024, (11 + r) * 8192, AUX_SC1);
#pragma unroll
  for (int a = 0; a < 2; a++) gate[a] = __builtin_bit_cast(float, __builtin_amdgcn_raw_buffer_load_b32(rs_ws, WS_G + (tgi * 32 + 16 * a + j) * 4, 0, AUX_SC1));
  __builtin_amdgcn_sched_barrier(0);
#pragma unroll
  for (int r = 0; r < 4; r++)
#pragma unroll
    for (int c = 0; c < 4; c++) {
      const v4u wf = trr2(lds + (r < 3 ? (r + 1) * PAGE + ab[c] : 4 * PAGE + (w - q) * 4096 + ab[c]), 1024);
#pragma unroll
      for (int a = 0; a < 2; a++) oa[a][c] = mfma(wf, of[r][a], oa[a][c]);
    }
  STAMP(11);
#pragma unroll
  for (int a = 0; a < 2; a++)
#pragma unroll
    for (int c = 0; c < 4; c++) {
      f32x4 v;
#pragma unroll
      for (int r = 0; r < 4; r++) {
        const unsigned bb = b2r[c][r >> 1];
        const float bias = q == 0 ? ((r & 1) ? bfhi(bb) : bflo(bb)) : 0.f;
        v[r] = gate[a] * oa[a][c][r] + (1.f - gate[a]) * (ob[a][c][r] + bias);
      }
      *(LAS f32x4*)(lds + RED + (q * 64 + 32 * th + 16 * a + j) * 256 + (((4 * c + g) ^ (j & 7)) * 16)) = v;
    }
  LBAR();
  {
    const int tok = tid >> 3, c8 = tid & 7;
    f32x4 s0 = {0.f, 0.f, 0.f, 0.f}, s1 = {0.f, 0.f, 0.f, 0.f};
#pragma unroll
    for (int qq = 0; qq < 4; qq++) {
      s0 += *(LAS f32x4*)(lds + RED + (qq * 64 + tok) * 256 + (((2 * c8) ^ (tok & 7)) * 16));
      s1 += *(LAS f32x4*)(lds + RED + (qq * 64 + tok) * 256 + (((2 * c8 + 1) ^ (tok & 7)) * 16));
    }
    *(GAS v4u*)((GAS char*)p.out + ((panel * 64 + tok) * 512 + ct * 64 + c8 * 8) * 2) = pack8(s0, s1);
  }
  STAMP(12);
}
}

extern "C" void kernel_launch(void* const* d_in, const int* in_sizes, int n_in, void* d_out, int out_size, void* d_ws, size_t ws_size,
                              hipStream_t stream) {
  gm::Params p;
  p.x = d_in[0]; p.wqkv = d_in[1]; p.wphi = d_in[2]; p.wo = d_in[3];
  p.w1 = d_in[5]; p.b1 = d_in[6]; p.w2 = d_in[7]; p.b2 = d_in[8]; p.gw = d_in[9];
  p.out = d_out; p.ws = d_ws;
  gm::fused<<<256, 512, 0, stream>>>(p);
}
```

```cpp
#include <hip/hip_runtime.h>
#include <hip/hip_bf16.h>

typedef unsigned long long u64;
typedef unsigned v4u __attribute__((ext_vector_type(4)));
typedef unsigned v2u __attribute__((ext_vector_type(2)));
typedef float f32x4 __attribute__((ext_vector_type(4)));
typedef float f32x2 __attribute__((ext_vector_type(2)));
typedef __bf16 bf16x8 __attribute__((ext_vector_type(8)));
typedef __bf16 bf16x2 __attribute__((ext_vector_type(2)));
typedef short s16x4 __attribute__((ext_vector_type(4)));
#define GAS __attribute__((address_space(1)))
#define LAS __attribute__((address_space(3)))
#define DI __device__ __forceinline__
#define RLX_AGENT __ATOMIC_RELAXED, __HIP_MEMORY_SCOPE_AGENT

#define STAMPS 0

namespace gm {
constexpr int S = 2048, D = 512, F = 1376;
constexpr unsigned WS_E = 0, WS_KVF = 4096, WS_HF = 12288, WS_OF = 20480, WS_G = 32768, WS_ZP = 40960, WS_KVP = 131072,
                   WS_ACT = 2u << 20, ACT_BYTES = 16u * 4 * 60 * 2048, WS_END = WS_ACT + ACT_BYTES, WS_STAMP = 16u << 20;
constexpr int SLOT = 40960, WOFF = 16384, PAGE = 16384;
constexpr int IMG = 73728;
constexpr int KVR = 9 * PAGE;
constexpr int KVR_ZERO = KVR + 4 * 17 * 64;
constexpr int STAT = 7 * PAGE;
constexpr int RED = 6 * PAGE;
constexpr int AUX_SC1 = 16;
constexpr unsigned SPIN_CAP = 1u << 22;

struct Params {
  const void *x, *wqkv, *wphi, *wo, *w1, *b1, *w2, *b2, *gw;
  void *out, *ws;
};

DI float asf(unsigned u) { return __builtin_bit_cast(float, u); }
DI unsigned asu(float f) { return __builtin_bit_cast(unsigned, f); }
DI bf16x2 asb2(unsigned u) { return __builtin_bit_cast(bf16x2, u); }
DI f32x4 mfma(v4u a, v4u b, f32x4 c) {
  return __builtin_amdgcn_mfma_f32_16x16x32_bf16(__builtin_bit_cast(bf16x8, a), __builtin_bit_cast(bf16x8, b), c, 0, 0, 0);
}
DI unsigned pk(float a, float b) { return __builtin_bit_cast(unsigned, __builtin_convertvector((f32x2){a, b}, bf16x2)); }
DI float bflo(unsigned u) { return __builtin_bit_cast(float, u << 16); }
DI float bfhi(unsigned u) { return __builtin_bit_cast(float, u & 0xffff0000u); }
DI v2u trr(LAS char* p) { return __builtin_bit_cast(v2u, __builtin_amdgcn_ds_read_tr16_b64_v4i16((LAS s16x4*)p)); }
DI v4u trr2(LAS char* p, int off2) { v2u a = trr(p), b = trr(p + off2); return (v4u){a[0], a[1], b[0], b[1]}; }
DI f32x2 pair16(float v) { auto r = __builtin_amdgcn_permlane16_swap(asu(v), asu(v), false, false); return (f32x2){asf(r[0]), asf(r[1])}; }
DI f32x2 pair32(float v) { auto r = __builtin_amdgcn_permlane32_swap(asu(v), asu(v), false, false); return (f32x2){asf(r[0]), asf(r[1])}; }
DI float sum16(float v) { const f32x2 r = pair16(v); return r[0] + r[1]; }
DI float sum32(float v) { const f32x2 r = pair32(v); return r[0] + r[1]; }
DI float max16(float v) { const f32x2 r = pair16(v); return fmaxf(r[0], r[1]); }
DI float min16(float v) { const f32x2 r = pair16(v); return fminf(r[0], r[1]); }
#define WAITV(n) asm volatile("s_waitcnt vmcnt(%0)" ::"n"(n) : "memory")
#define WAITL() asm volatile("s_waitcnt lgkmcnt(0)" ::: "memory")
#define BAR() do { asm volatile("" ::: "memory"); __builtin_amdgcn_s_barrier(); asm volatile("" ::: "memory"); } while (0)
#define LBAR() do { WAITL(); __builtin_amdgcn_s_barrier(); asm volatile("" ::: "memory"); } while (0)

DI void glds(__amdgpu_buffer_rsrc_t rs, LAS char* dst, unsigned voff, unsigned soff) {
  asm volatile("s_mov_b32 m0, %0\n\ts_nop 0\n\tbuffer_load_dwordx4 %1, %2, %3 offen lds" ::"s"((unsigned)(size_t)dst), "v"(voff), "s"(rs), "s"(soff) : "memory");
}
DI void dual_softmax(const f32x4& p0, const f32x4& p1, v4u& plus, v4u& minus) {
  float mx = fmaxf(fmaxf(fmaxf(p0[0], p0[1]), fmaxf(p0[2], p0[3])), fmaxf(fmaxf(p1[0], p1[1]), fmaxf(p1[2], p1[3])));
  float mn = fminf(fminf(fminf(p0[0], p0[1]), fminf(p0[2], p0[3])), fminf(fminf(p1[0], p1[1]), fminf(p1[2], p1[3])));
  mx = max16(mx); mn = min16(mn);
  constexpr float L2E = 1.4426950408889634f;
  const float mxs = -mx * L2E, mns = mn * L2E;
  float ep[8], em[8], sp = 0.f, sm = 0.f;
#pragma unroll
  for (int e = 0; e < 8; e++) {
    const float p = e < 4 ? p0[e & 3] : p1[e & 3];
    ep[e] = __builtin_amdgcn_exp2f(__builtin_fmaf(p, L2E, mxs));
    em[e] = __builtin_amdgcn_exp2f(__builtin_fmaf(p, -L2E, mns));
    sp += ep[e]; sm += em[e];
  }
  sp = __builtin_amdgcn_rcpf(sum16(sp)); sm = __builtin_amdgcn_rcpf(sum16(sm));
#pragma unroll
  for (int e = 0; e < 4; e++) { plus[e] = pk(ep[2 * e] * sp, ep[2 * e + 1] * sp); minus[e] = pk(em[2 * e] * sm, em[2 * e + 1] * sm); }
}
DI v4u pack8(const f32x4& a, const f32x4& b) { return (v4u){pk(a[0], a[1]), pk(a[2], a[3]), pk(b[0], b[1]), pk(b[2], b[3])}; }
DI void wait16(const GAS u64* line, u64 tag, int lane) {
#pragma nounroll
  for (unsigned spins = 0; spins < SPIN_CAP; ++spins) {
    const u64 v = __hip_atomic_load(line + (lane & 15), RLX_AGENT);
    if (__all(v == tag)) break;
    __builtin_amdgcn_s_sleep(1);
  }
  __builtin_amdgcn_fence(__ATOMIC_ACQUIRE, "wavefront");
}
DI void publish(GAS u64* flag, u64 tag, int lane) {
  WAITV(0);
  if (lane == 0) __hip_atomic_store(flag, tag, RLX_AGENT);
}

#define STAMP(i) do {} while (0)

__global__ void __launch_bounds__(512, 2) fused(Params p) {
  __shared__ __attribute__((aligned(16))) char smem[163840];
  LAS char* lds = (LAS char*)smem;
  const int tid = threadIdx.x, lane = tid & 63, w = __builtin_amdgcn_readfirstlane(tid >> 6);
  const int g = lane >> 4, j = lane & 15, q4 = (lane & 15) >> 2, p4 = lane & 3;
  const int tg = w >> 1, ch = w & 1;
  const int b = blockIdx.x, xcd = b & 7, idx = b >> 3;
  const int rt = (xcd >> 2) * 8 + (idx >> 2), c4 = idx & 3;
  const bool isM = c4 >= 2;
  const int grp = (xcd & 3) * 2 + (c4 & 1);
  const int u0 = grp < 3 ? 6 * grp : 18 + 5 * (grp - 3);
  const int rot = idx >> 2;
  const int panel = xcd * 4 + (idx >> 3), ct = idx & 7;
  STAMP(0);

  const __amdgpu_buffer_rsrc_t rs_ws = __builtin_amdgcn_make_buffer_rsrc(p.ws, 0, WS_END, 0x00020000);
  const __amdgpu_buffer_rsrc_t rs_x = __builtin_amdgcn_make_buffer_rsrc((void*)p.x, 0, S * D * 2, 0x00020000);
  const __amdgpu_buffer_rsrc_t rs_w = __builtin_amdgcn_make_buffer_rsrc((void*)(isM ? p.w1 : p.wqkv), 0, isM ? D * F * 2 : D * 1536 * 2, 0x00020000);
  const __amdgpu_buffer_rsrc_t rs_w2 = __builtin_amdgcn_make_buffer_rsrc((void*)p.w2, 0, F * D * 2, 0x00020000);
  const __amdgpu_buffer_rsrc_t rs_wo = __builtin_amdgcn_make_buffer_rsrc((void*)p.wo, 0, D * D * 2, 0x00020000);
  GAS char* wsp = (GAS char*)p.ws;

  {
    unsigned long long pc;
    asm volatile("s_getpc_b64 %0" : "=s"(pc));
    pc &= ~255ull;
    if (w < 2) {
      const GAS char* a = (const GAS char*)pc + (w * 32 + idx) * 1024 + lane * 16;
      if (w * 32 + idx < 32) asm volatile("s_mov_b32 m0, %0\n\ts_nop 0\n\tglobal_load_lds_dwordx4 %1, off" ::"s"(2 * w * 1024), "v"(a) : "memory");
    }
    if (w >= 2) {
      const GAS char* a;
      if (w == 2) a = (const GAS char*)p.w2 + ct * 128;
      if (w == 3) a = (const GAS char*)p.wo + ct * 128;
      if (w == 4) a = (const GAS char*)p.ws + WS_ACT + (rt * 4 * 60) * 2048;
      if (w == 5) a = (const GAS char*)p.ws + WS_ACT + (panel * 2 * 60 + 43) * 2048;
      if (w == 6) a = (const GAS char*)p.out + panel * 65536;
      if (w == 7) a = (const GAS char*)p.ws + WS_ACT - 4096;
      if (w == 4) a += 43 * 2048;
      asm volatile("s_mov_b32 m0, %0\n\ts_nop 0\n\tglobal_load_lds_dwordx4 %1, off" ::"s"(2 * w * 1024), "v"(a + lane * 16) : "memory");
    }
  }
  v2u ev;
  asm volatile("global_load_dwordx2 %0, %1, off sc1" : "=v"(ev) : "v"(wsp + WS_E) : "memory");
  v4u aux[16]; v2u b1r[6]; v4u wph;

  unsigned xv[2], wv[3];
#pragma unroll
  for (int i = 0; i < 2; i++) { const int row = 8 * (2 * w + i) + (lane >> 3); xv[i] = (rt * 128 + row) * 1024 + (((lane & 7) ^ ((row >> 1) & 7)) * 16); }
#pragma unroll
  for (int i = 0; i < 3; i++) {
    const int n = (3 * w + i) * 64 + lane, k = n / 24, cp = n % 24;
    const int c = cp ^ (((k >> 1) & 1) | (((k >> 3) & 1) << 2));
    const int seg = c >> 3;
    wv[i] = isM ? k * 2752 + u0 * 64 + c * 16 : k * 3072 + (seg == 2 ? 0 : (seg + 1) * 1024) + grp * 128 + (c & 7) * 16;
  }
  const unsigned wstep = isM ? 64 * 2752 : 64 * 3072;
  const unsigned w2v = (16 * w + (lane >> 3)) * 1024 + ct * 128 + (((lane & 7) ^ ((lane >> 3) & 6)) * 16);
  auto stage = [&](int t) {
    const int kt = (t + rot) & 7;
    LAS char* dst = lds + (t & 3) * SLOT;
    glds(rs_x, dst + (2 * w) * 1024, xv[0], kt * 128);
    glds(rs_x, dst + (2 * w + 1) * 1024, xv[1], kt * 128);
#pragma unroll
    for (int i = 0; i < 3; i++) glds(rs_w, dst + WOFF + (3 * w + i) * 1024, wv[i], kt * wstep);
  };
  auto round_ld = [&](int r, int page) {
    LAS char* dst = lds + page * PAGE + w * 2048;
    if (r < 11) { glds(rs_w2, dst, w2v, r * 131072); glds(rs_w2, dst + 1024, w2v + 8192, r * 131072); }
    else { glds(rs_wo, dst, w2v, (r - 11) * 131072); glds(rs_wo, dst + 1024, w2v + 8192, (r - 11) * 131072); }
  };

  { for (int t = 0; t < 8; t++) stage(t);
    for (int r = 0; r < 15; r++) round_ld(r, r % 10);
    WAITV(0);
    const unsigned long long t0 = __builtin_amdgcn_s_memtime(); while (__builtin_amdgcn_s_memtime() - t0 < 20000ull) __builtin_amdgcn_s_sleep(1);
    BAR(); }
  const int xb0 = (32 * tg + j) * 128 + ((g ^ (j >> 1)) * 16);
  const int sk = ((q4 >> 1) & 1) | ((g & 1) << 2);
  const int wrow = WOFF + (8 * g + q4) * 384 + 8 * (p4 & 1);
  const int wb0 = wrow + 16 * ((4 * ch + 2 * (p4 >> 1)) ^ sk), wb1 = wrow + 16 * ((4 * ch + 2 * (p4 >> 1) + 1) ^ sk);
  const int cs1 = tg < 2 ? tg + 4 : tg;
  const int cb0 = ((tg & 1) ? wb1 : wb0) + (tg >> 1) * 128, cb1 = ((cs1 & 1) ? wb1 : wb0) + (cs1 >> 1) * 128;

  f32x4 acc[2][6];
#pragma unroll
  for (int a = 0; a < 2; a++)
#pragma unroll
    for (int t = 0; t < 6; t++) acc[a][t] = (f32x4){0.f, 0.f, 0.f, 0.f};
  f32x4 cs[2] = {{0.f, 0.f, 0.f, 0.f}, {0.f, 0.f, 0.f, 0.f}}, ax = {0.f, 0.f, 0.f, 0.f};
  float ssq = 0.f;
  const v4u ones = {0x3F803F80u, 0x3F803F80u, 0x3F803F80u, 0x3F803F80u};

  auto kloop = [&](auto ism) {
    constexpr bool M = decltype(ism)::value;
    v4u xf[2][2], wf[2][6], cf[2][2];
    if (M) {
      const __amdgpu_buffer_rsrc_t rs_b1 = __builtin_amdgcn_make_buffer_rsrc((void*)p.b1, 0, F * 2, 0x00020000);
#pragma unroll
      for (int t = 0; t < 6; t++) {
        const unsigned off = ((u0 + 2 * (t >> 1) + ch) * 32 + 16 * (g >> 1) + 8 * (t & 1) + 4 * (g & 1)) * 2;
        asm volatile("buffer_load_dwordx2 %0, %1, %2, 0 offen" : "=v"(b1r[t]) : "v"(off), "s"(rs_b1) : "memory");
      }
    } else {
      const GAS char* a = (const GAS char*)p.wphi + (4 * grp + 2 * ch) * 512 + lane * 16;
      asm volatile("global_load_dwordx4 %0, %1, off" : "=v"(wph) : "v"(a) : "memory");
      const __amdgpu_buffer_rsrc_t rs_gw = __builtin_amdgcn_make_buffer_rsrc((void*)p.gw, 0, D * 2, 0x00020000);
#pragma unroll
      for (int s = 0; s < 16; s++)
        asm volatile("buffer_load_dwordx4 %0, %1, %2, %3 offen" : "=v"(aux[s]) : "v"(16 * g), "s"(rs_gw), "s"(((((s >> 1) + rot) & 7) * 64 + (s & 1) * 32) * 2) : "memory");
    }
    stage(0); stage(1); stage(2); stage(3);
    auto rd = [&](int t, int ks) {
      LAS char* sl = lds + (t & 3) * SLOT;
#pragma unroll
      for (int a = 0; a < 2; a++) xf[ks][a] = *(LAS v4u*)(sl + (xb0 ^ (ks * 64)) + a * 2048);
#pragma unroll
      for (int t6 = 0; t6 < 6; t6++) wf[ks][t6] = trr2(sl + ((t6 & 1) ? wb1 : wb0) + (t6 >> 1) * 128 + ks * 12288, 1536);
      if (M) { cf[ks][0] = trr2(sl + cb0 + ks * 12288, 1536); cf[ks][1] = trr2(sl + cb1 + ks * 12288, 1536); }
    };
    auto mm = [&](int ks, int t0, int t1) {
#pragma unroll
      for (int t6 = t0; t6 < t1; t6++)
#pragma unroll
        for (int a = 0; a < 2; a++) acc[a][t6] = mfma(wf[ks][t6], xf[ks][a], acc[a][t6]);
    };
    auto extra = [&](int i, int ks) {
      const v4u xs = ch ? xf[ks][1] : xf[ks][0];
      if (M) {
        cs[0] = mfma(cf[ks][0], ones, cs[0]); cs[1] = mfma(cf[ks][1], ones, cs[1]);
        ax = mfma(ones, xs, ax);
#pragma unroll
        for (int e = 0; e < 4; e++) ssq = __builtin_amdgcn_fdot2_f32_bf16(asb2(xs[e]), asb2(xs[e]), ssq, false);
        asm volatile("" : "+v"(cs[0]), "+v"(cs[1]), "+v"(ssq));
      } else ax = mfma(aux[2 * i + ks], xs, ax);
      asm volatile("" : "+v"(ax));
    };
    STAMP(21);
    WAITV(15);
    asm volatile("" : "+v"(ev));
    if (M) {
#pragma unroll
      for (int t = 0; t < 6; t++) asm volatile("" : "+v"(b1r[t]));
    } else {
      asm volatile("" : "+v"(wph));
#pragma unroll
      for (int s = 0; s < 16; s++) asm volatile("" : "+v"(aux[s]));
    }
    BAR();
    STAMP(13);
    rd(0, 0);
#pragma unroll
    for (int i = 0; i < 8; i++) {
      rd(i, 1);
      __builtin_amdgcn_sched_barrier(0);
      mm(0, 0, 6); extra(i, 0);
      mm(1, 0, 3); extra(i, 1);
      __builtin_amdgcn_sched_barrier(0);
      if (i < 7) {
        if (i < 5) WAITV(10); else if (i == 5) WAITV(9); else WAITV(8);
        LBAR();
        STAMP(14 + i);
        rd(i + 1, 0);
        if (i < 4) stage(i + 4);
        if (i == 4) { round_ld(0, 0); round_ld(1, 1); }
        if (i == 5) { round_ld(2, 2); round_ld(3, 3); }
        if (i == 6 && M) { round_ld(4, 4); round_ld(5, 5); round_ld(6, 6); }
        __builtin_amdgcn_sched_barrier(0);
      }
      mm(1, 3, 6);
    }
  };

  u64 tag;
  v4u phq[2][2];
  if (isM) {
    kloop(std::true_type{});
    STAMP(1);
    { u64 z = (((u64)ev[1] << 32) | ev[0]) + 0x9E3779B97F4A7C15ull; z = (z ^ (z >> 30)) * 0xBF58476D1CE4E5B9ull; z = (z ^ (z >> 27)) * 0x94D049BB133111EBull; tag = z ^ (z >> 31); }
    ssq = sum32(sum16(ssq));
    if (g == 0) {
      const float mean = ax[0] * (1.f / D);
      const float var = fmaxf(ssq * (1.f / D) - mean * mean, 0.f);
      *(LAS f32x2*)(lds + STAT + (w * 16 + j) * 8) = (f32x2){mean, __builtin_amdgcn_rsqf(var + 1e-5f)};
    }
    if (j == 0) {
      *(LAS f32x4*)(lds + STAT + 1024 + ((ch * 6 + tg) * 16 + 4 * g) * 4) = cs[0];
      *(LAS f32x4*)(lds + STAT + 1024 + ((ch * 6 + cs1) * 16 + 4 * g) * 4) = cs[1];
    }
    LBAR();
    round_ld(8, 8); round_ld(9, 9);
    f32x2 st[2];
#pragma unroll
    for (int a = 0; a < 2; a++) st[a] = *(LAS f32x2*)(lds + STAT + ((tg * 2 + a) * 16 + j) * 8);
#pragma unroll
    for (int sgm = 0; sgm < 3; sgm++) {
      f32x4 csv[2];
#pragma unroll
      for (int t2 = 0; t2 < 2; t2++) csv[t2] = *(LAS f32x4*)(lds + STAT + 1024 + ((ch * 6 + sgm * 2 + t2) * 16 + 4 * g) * 4);
      const int lu = 2 * sgm + ch;
#pragma unroll
      for (int a = 0; a < 2; a++) {
        f32x4 hv[2];
#pragma unroll
        for (int t2 = 0; t2 < 2; t2++)
#pragma unroll
          for (int r = 0; r < 4; r++) {
            const unsigned bb = b1r[sgm * 2 + t2][r >> 1];
            const float bias = (r & 1) ? bfhi(bb) : bflo(bb);
            const float v = __builtin_fmaf(__builtin_fmaf(-st[a][0], csv[t2][r], acc[a][sgm * 2 + t2][r]), st[a][1], bias);
            const float u = v * __builtin_fmaf(v * v, -0.044715f * 2.302208198f, -2.302208198f);
            hv[t2][r] = v * __builtin_amdgcn_rcpf(1.f + __builtin_amdgcn_exp2f(u));
          }
        if (lu < 5 || grp < 3)
          __builtin_amdgcn_raw_buffer_store_b128(pack8(hv[0], hv[1]), rs_ws, WS_ACT + (((rt * 4 + tg) * 60 + u0 + lu) * 2 + a) * 1024 + lane * 16, 0, AUX_SC1);
      }
    }
    STAMP(2);
    publish((GAS u64*)(wsp + WS_HF) + ((rt * 4 + tg) * 16 + grp * 2 + ch), tag, lane);
    STAMP(3);
  } else {
    kloop(std::false_type{});
    STAMP(1);
    { u64 z = (((u64)ev[1] << 32) | ev[0]) + 0x9E3779B97F4A7C15ull; z = (z ^ (z >> 30)) * 0xBF58476D1CE4E5B9ull; z = (z ^ (z >> 27)) * 0x94D049BB133111EBull; tag = z ^ (z >> 31); }
    LAS char* mine = lds + IMG + (2 * ch * 128 + 32 * tg) * 96;
    *(LAS v4u*)(mine + lane * 16) = wph;
    v4u wp[2];
    const bool act = (j >> 3) == (g >> 1);
#pragma unroll
    for (int m = 0; m < 2; m++) {
      wp[m] = trr2(mine + ((g >> 1) * 16 + 4 * (g & 1) + q4) * 32 + (8 * m + 4 * (p4 & 1)) * 2, 256);
#pragma unroll
      for (int e = 0; e < 4; e++) wp[m][e] = act ? wp[m][e] : 0u;
    }
    const f32x4 zero = {0.f, 0.f, 0.f, 0.f};
#pragma unroll
    for (int a = 0; a < 2; a++) {
      const v4u kf = pack8(acc[a][0], acc[a][1]);
      v4u pl, mi;
      dual_softmax(mfma(wp[0], kf, zero), mfma(wp[1], kf, zero), pl, mi);
      LAS char* row = lds + IMG + ((2 * ch + (g >> 1)) * 128 + 32 * tg + 16 * a + j) * 96;
      *(LAS v4u*)(row + 16 * (g & 1)) = pl;
      *(LAS v4u*)(row + 32 + 16 * (g & 1)) = mi;
      *(LAS v2u*)(row + 64 + 8 * (g & 1)) = (v2u){pk(acc[a][2][0], acc[a][2][1]), pk(acc[a][2][2], acc[a][2][3])};
      *(LAS v2u*)(row + 80 + 8 * (g & 1)) = (v2u){pk(acc[a][3][0], acc[a][3][1]), pk(acc[a][3][2], acc[a][3][3])};
    }
    LBAR();
    f32x4 kv = zero, zz = zero;
    LAS char* ib = lds + IMG + ((w >> 1) * 128 + 4 * g + q4) * 96 + 8 * p4;
#pragma unroll
    for (int ks = 0; ks < 4; ks++) {
      const v4u pa = trr2(ib + ks * 32 * 96 + 32 * (w & 1), 16 * 96), vb = trr2(ib + ks * 32 * 96 + 64, 16 * 96);
      kv = mfma(pa, vb, kv); zz = mfma(pa, ones, zz);
    }
    __builtin_amdgcn_raw_buffer_store_b128(__builtin_bit_cast(v4u, kv), rs_ws, WS_KVP + ((grp * 16 + rt) * 8 + w) * 1024 + lane * 16, 0, AUX_SC1);
    if (j == 0) __builtin_amdgcn_raw_buffer_store_b128(__builtin_bit_cast(v4u, zz), rs_ws, WS_ZP + ((grp * 8 + w) * 16 + rt) * 64 + g * 16, 0, AUX_SC1);
    STAMP(2);
    publish((GAS u64*)(wsp + WS_KVF) + ((grp * 8 + w) * 16 + rt), tag, lane);
    STAMP(3);
#pragma unroll
    for (int a = 0; a < 2; a++) {
      const v4u qf = pack8(acc[a][4], acc[a][5]);
      dual_softmax(mfma(wp[0], qf, zero), mfma(wp[1], qf, zero), phq[a][0], phq[a][1]);
    }
    if (g == 0 && grp == 0) {
      const float gate = __builtin_amdgcn_rcpf(1.f + __builtin_amdgcn_exp2f(-1.4426950408889634f * ax[0]));
      __builtin_amdgcn_raw_buffer_store_b32(__builtin_bit_cast(unsigned, gate), rs_ws, WS_G + (rt * 128 + w * 16 + j) * 4, 0, AUX_SC1);
    }
  }

  const int th = w >> 2, q = w & 3;
  int ab[4];
  { const int kk = 16 * (g >> 1) + 4 * (g & 1) + q4, u6 = kk & 6;
#pragma unroll
    for (int c = 0; c < 4; c++) ab[c] = q * 4096 + kk * 128 + (((2 * c) ^ u6) + (p4 >> 1)) * 16 + 8 * (p4 & 1); }
  BAR();
  v4u wt[4][4];
#pragma unroll
  for (int r = 0; r < 4; r++)
#pragma unroll
    for (int c = 0; c < 4; c++) wt[r][c] = trr2(lds + r * PAGE + ab[c], 1024);
  LBAR();
  if (isM) round_ld(7, 7);
  else { round_ld(4, 4); round_ld(5, 5); round_ld(6, 6); round_ld(7, 7); round_ld(8, 8); }
  round_ld(10, 0); round_ld(11, 1); round_ld(12, 2); round_ld(13, 3);
  STAMP(4);

  if (!isM) {
    wait16((const GAS u64*)(wsp + WS_KVF) + (grp * 8 + w) * 16, tag, lane);
    STAMP(5);
    v4u pt[16];
#pragma unroll
    for (int r = 0; r < 16; r++) pt[r] = __builtin_amdgcn_raw_buffer_load_b128(rs_ws, WS_KVP + (grp * 16 * 8 + w) * 1024 + lane * 16, r * 8192, AUX_SC1);
    const v4u zr = __builtin_amdgcn_raw_buffer_load_b128(rs_ws, WS_ZP + (grp * 8 + w) * 1024 + lane * 16, 0, AUX_SC1);
    __builtin_amdgcn_sched_barrier(0);
    f32x4 kv = __builtin_bit_cast(f32x4, pt[0]);
#pragma unroll
    for (int r = 1; r < 16; r++) kv += __builtin_bit_cast(f32x4, pt[r]);
    *(LAS v2u*)(lds + KVR + ((w >> 1) * 17 + j) * 64 + (16 * (w & 1) + 4 * g) * 2) = (v2u){pk(kv[0], kv[1]), pk(kv[2], kv[3])};
    f32x4 z = __builtin_bit_cast(f32x4, zr);
#pragma unroll
    for (int e = 0; e < 4; e++) {
      float v = z[e];
      v += __builtin_bit_cast(float, __builtin_amdgcn_update_dpp(0u, __builtin_bit_cast(unsigned, v), 0x124, 0xf, 0xf, false));
      v += __builtin_bit_cast(float, __builtin_amdgcn_update_dpp(0u, __builtin_bit_cast(unsigned, v), 0x128, 0xf, 0xf, false));
      z[e] = sum32(sum16(v));
    }
    if (lane < 4) *(LAS v2u*)(lds + KVR + ((w >> 1) * 17 + 16) * 64 + (16 * (w & 1) + 4 * lane) * 2) = (v2u){pk(z[0], z[1]), pk(z[2], z[3])};
    if (tid == 0) *(LAS v4u*)(lds + KVR_ZERO) = (v4u){0u, 0u, 0u, 0u};
    LBAR();
    STAMP(6);
    const bool act = (j >> 3) == (g >> 1);
    const int hb = KVR + (2 * ch + (g >> 1)) * 17 * 64 + 16 * (g & 1);
    v4u ka[2][2], za[2];
#pragma unroll
    for (int sg = 0; sg < 2; sg++) {
#pragma unroll
      for (int dh = 0; dh < 2; dh++) ka[sg][dh] = *(LAS v4u*)(lds + (act ? hb + (8 * dh + (j & 7)) * 64 + 32 * sg : KVR_ZERO));
      za[sg] = *(LAS v4u*)(lds + (act ? hb + 16 * 64 + 32 * sg : KVR_ZERO));
    }
    const f32x4 zero = {0.f, 0.f, 0.f, 0.f};
#pragma unroll
    for (int a = 0; a < 2; a++) {
      f32x4 n0 = mfma(ka[1][0], phq[a][1], mfma(ka[0][0], phq[a][0], zero));
      f32x4 n1 = mfma(ka[1][1], phq[a][1], mfma(ka[0][1], phq[a][0], zero));
      const f32x4 dn = mfma(za[1], phq[a][1], mfma(za[0], phq[a][0], zero));
      const float rd = 1.f / (dn[0] + 1e-6f);
      n0 *= rd; n1 *= rd;
      __builtin_amdgcn_raw_buffer_store_b128(pack8(n0, n1), rs_ws, WS_ACT + (((rt * 4 + tg) * 60 + 44 + 2 * grp + ch) * 2 + a) * 1024 + lane * 16, 0, AUX_SC1);
    }
    publish((GAS u64*)(wsp + WS_OF) + ((rt * 4 + tg) * 16 + grp * 2 + ch), tag, lane);
    STAMP(7);
    BAR();
    round_ld(9, 9);
  }

  const int tgi = panel * 2 + th;
  wait16((const GAS u64*)(wsp + WS_HF) + tgi * 16, tag, lane);
  STAMP(8);
  const unsigned av = WS_ACT + (tgi * 60 + q) * 2048 + lane * 16;
  v4u bf[11][2];
#pragma unroll
  for (int r = 0; r < 11; r++)
#pragma unroll
    for (int a = 0; a < 2; a++)
      bf[r][a] = __builtin_amdgcn_raw_buffer_load_b128(rs_ws, av + a * 1024, (r == 10 && q == 3) ? WS_END : r * 8192, AUX_SC1);
  v2u b2r[4];
#pragma unroll
  for (int c = 0; c < 4; c++) b2r[c] = *(const GAS v2u*)((const GAS char*)p.b2 + (ct * 64 + 16 * c + 4 * g) * 2);
  __builtin_amdgcn_sched_barrier(0);
  f32x4 ob[2][4], oa[2][4];
#pragma unroll
  for (int a = 0; a < 2; a++)
#pragma unroll
    for (int c = 0; c < 4; c++) { ob[a][c] = (f32x4){0.f, 0.f, 0.f, 0.f}; oa[a][c] = (f32x4){0.f, 0.f, 0.f, 0.f}; }
#pragma unroll
  for (int r = 0; r < 11; r++) {
    if (r == 7) BAR();
#pragma unroll
    for (int c = 0; c < 4; c++) {
      const v4u wf = r < 4 ? wt[r < 4 ? r : 0][c] : trr2(lds + (r < 10 ? r : 0) * PAGE + ab[c], 1024);
#pragma unroll
      for (int a = 0; a < 2; a++) ob[a][c] = mfma(wf, bf[r][a], ob[a][c]);
    }
  }
  STAMP(9);
  BAR();
  {
    const unsigned pv = (32 * q + (lane >> 3)) * 1024 + ct * 128 + (((lane & 7) ^ ((lane >> 3) & 6)) * 16);
#pragma unroll
    for (int i = 0; i < 4; i++) glds(rs_wo, lds + 4 * PAGE + w * 4096 + i * 1024, pv + i * 8192, 3 * 131072);
  }
  if (b == 0 && w == 7) {
#pragma nounroll
    for (unsigned spins = 0; spins < SPIN_CAP; ++spins) {
      bool ok = true;
#pragma unroll
      for (int i = 0; i < 16; i++) ok &= __hip_atomic_load((const GAS u64*)(wsp + WS_HF) + i * 64 + lane, RLX_AGENT) == tag;
      if (__all(ok)) break;
      __builtin_amdgcn_s_sleep(1);
    }
  }
  wait16((const GAS u64*)(wsp + WS_OF) + tgi * 16, tag, lane);
  STAMP(10);
  if (b == 0 && w == 7 && lane == 0) __hip_atomic_store((GAS u64*)(wsp + WS_E), tag, RLX_AGENT);
  float gate[2];
  v4u of[4][2];
#pragma unroll
  for (int r = 0; r < 4; r++)
#pragma unroll
    for (int a = 0; a < 2; a++) of[r][a] = __builtin_amdgcn_raw_buffer_load_b128(rs_ws, av + a * 1024, (11 + r) * 8192, AUX_SC1);
#pragma unroll
  for (int a = 0; a < 2; a++) gate[a] = __builtin_bit_cast(float, __builtin_amdgcn_raw_buffer_load_b32(rs_ws, WS_G + (tgi * 32 + 16 * a + j) * 4, 0, AUX_SC1));
  __builtin_amdgcn_sched_barrier(0);
#pragma unroll
  for (int r = 0; r < 4; r++)
#pragma unroll
    for (int c = 0; c < 4; c++) {
      const v4u wf = trr2(lds + (r < 3 ? (r + 1) * PAGE + ab[c] : 4 * PAGE + (w - q) * 4096 + ab[c]), 1024);
#pragma unroll
      for (int a = 0; a < 2; a++) oa[a][c] = mfma(wf, of[r][a], oa[a][c]);
    }
  STAMP(11);
#pragma unroll
  for (int a = 0; a < 2; a++)
#pragma unroll
    for (int c = 0; c < 4; c++) {
      f32x4 v;
#pragma unroll
      for (int r = 0; r < 4; r++) {
        const unsigned bb = b2r[c][r >> 1];
        const float bias = q == 0 ? ((r & 1) ? bfhi(bb) : bflo(bb)) : 0.f;
        v[r] = gate[a] * oa[a][c][r] + (1.f - gate[a]) * (ob[a][c][r] + bias);
      }
      *(LAS f32x4*)(lds + RED + (q * 64 + 32 * th + 16 * a + j) * 256 + (((4 * c + g) ^ (j & 7)) * 16)) = v;
    }
  LBAR();
  {
    const int tok = tid >> 3, c8 = tid & 7;
    f32x4 s0 = {0.f, 0.f, 0.f, 0.f}, s1 = {0.f, 0.f, 0.f, 0.f};
#pragma unroll
    for (int qq = 0; qq < 4; qq++) {
      s0 += *(LAS f32x4*)(lds + RED + (qq * 64 + tok) * 256 + (((2 * c8) ^ (tok & 7)) * 16));
      s1 += *(LAS f32x4*)(lds + RED + (qq * 64 + tok) * 256 + (((2 * c8 + 1) ^ (tok & 7)) * 16));
    }
    *(GAS v4u*)((GAS char*)p.out + ((panel * 64 + tok) * 512 + ct * 64 + c8 * 8) * 2) = pack8(s0, s1);
  }
  STAMP(12);
}
}

extern "C" void kernel_launch(void* const* d_in, const int* in_sizes, int n_in, void* d_out, int out_size, void* d_ws, size_t ws_size,
                              hipStream_t stream) {
  gm::Params p;
  p.x = d_in[0]; p.wqkv = d_in[1]; p.wphi = d_in[2]; p.wo = d_in[3];
  p.w1 = d_in[5]; p.b1 = d_in[6]; p.w2 = d_in[7]; p.b2 = d_in[8]; p.gw = d_in[9];
  p.out = d_out; p.ws = d_ws;
  gm::fused<<<256, 512, 0, stream>>>(p);
}
```

```cpp
#include <hip/hip_runtime.h>
#include <hip/hip_bf16.h>

typedef unsigned long long u64;
typedef unsigned v4u __attribute__((ext_vector_type(4)));
typedef unsigned v2u __attribute__((ext_vector_type(2)));
typedef float f32x4 __attribute__((ext_vector_type(4)));
typedef float f32x2 __attribute__((ext_vector_type(2)));
typedef __bf16 bf16x8 __attribute__((ext_vector_type(8)));
typedef __bf16 bf16x2 __attribute__((ext_vector_type(2)));
typedef short s16x4 __attribute__((ext_vector_type(4)));
#define GAS __attribute__((address_space(1)))
#define LAS __attribute__((address_space(3)))
#define DI __device__ __forceinline__
#define RLX_AGENT __ATOMIC_RELAXED, __HIP_MEMORY_SCOPE_AGENT

#define STAMPS 0

namespace gm {
constexpr int S = 2048, D = 512, F = 1376;
constexpr unsigned WS_E = 0, WS_KVF = 4096, WS_HF = 12288, WS_OF = 20480, WS_G = 32768, WS_ZP = 40960, WS_KVP = 131072,
                   WS_ACT = 2u << 20, ACT_BYTES = 16u * 4 * 60 * 2048, WS_END = WS_ACT + ACT_BYTES, WS_STAMP = 16u << 20;
constexpr int SLOT = 40960, WOFF = 16384, PAGE = 16384;
constexpr int IMG = 73728;
constexpr int KVR = 9 * PAGE;
constexpr int KVR_ZERO = KVR + 4 * 17 * 64;
constexpr int STAT = 7 * PAGE;
constexpr int RED = 6 * PAGE;
constexpr int AUX_SC1 = 16;
constexpr unsigned SPIN_CAP = 1u << 22;

struct Params {
  const void *x, *wqkv, *wphi, *wo, *w1, *b1, *w2, *b2, *gw;
  void *out, *ws;
};

DI float asf(unsigned u) { return __builtin_bit_cast(float, u); }
DI unsigned asu(float f) { return __builtin_bit_cast(unsigned, f); }
DI bf16x2 asb2(unsigned u) { return __builtin_bit_cast(bf16x2, u); }
DI f32x4 mfma(v4u a, v4u b, f32x4 c) {
  return __builtin_amdgcn_mfma_f32_16x16x32_bf16(__builtin_bit_cast(bf16x8, a), __builtin_bit_cast(bf16x8, b), c, 0, 0, 0);
}
DI unsigned pk(float a, float b) { return __builtin_bit_cast(unsigned, __builtin_convertvector((f32x2){a, b}, bf16x2)); }
DI float bflo(unsigned u) { return __builtin_bit_cast(float, u << 16); }
DI float bfhi(unsigned u) { return __builtin_bit_cast(float, u & 0xffff0000u); }
DI v2u trr(LAS char* p) { return __builtin_bit_cast(v2u, __builtin_amdgcn_ds_read_tr16_b64_v4i16((LAS s16x4*)p)); }
DI v4u trr2(LAS char* p, int off2) { v2u a = trr(p), b = trr(p + off2); return (v4u){a[0], a[1], b[0], b[1]}; }
DI f32x2 pair16(float v) { auto r = __builtin_amdgcn_permlane16_swap(asu(v), asu(v), false, false); return (f32x2){asf(r[0]), asf(r[1])}; }
DI f32x2 pair32(float v) { auto r = __builtin_amdgcn_permlane32_swap(asu(v), asu(v), false, false); return (f32x2){asf(r[0]), asf(r[1])}; }
DI float sum16(float v) { const f32x2 r = pair16(v); return r[0] + r[1]; }
DI float sum32(float v) { const f32x2 r = pair32(v); return r[0] + r[1]; }
DI float max16(float v) { const f32x2 r = pair16(v); return fmaxf(r[0], r[1]); }
DI float min16(float v) { const f32x2 r = pair16(v); return fminf(r[0], r[1]); }
#define WAITV(n) asm volatile("s_waitcnt vmcnt(%0)" ::"n"(n) : "memory")
#define WAITL() asm volatile("s_waitcnt lgkmcnt(0)" ::: "memory")
#define BAR() do { asm volatile("" ::: "memory"); __builtin_amdgcn_s_barrier(); asm volatile("" ::: "memory"); } while (0)
#define LBAR() do { WAITL(); __builtin_amdgcn_s_barrier(); asm volatile("" ::: "memory"); } while (0)

DI void glds(__amdgpu_buffer_rsrc_t rs, LAS char* dst, unsigned voff, unsigned soff) {
  asm volatile("s_mov_b32 m0, %0\n\ts_nop 0\n\tbuffer_load_dwordx4 %1, %2, %3 offen lds" ::"s"((unsigned)(size_t)dst), "v"(voff), "s"(rs), "s"(soff) : "memory");
}
DI void dual_softmax(const f32x4& p0, const f32x4& p1, v4u& plus, v4u& minus) {
  float mx = fmaxf(fmaxf(fmaxf(p0[0], p0[1]), fmaxf(p0[2], p0[3])), fmaxf(fmaxf(p1[0], p1[1]), fmaxf(p1[2], p1[3])));
  float mn = fminf(fminf(fminf(p0[0], p0[1]), fminf(p0[2], p0[3])), fminf(fminf(p1[0], p1[1]), fminf(p1[2], p1[3])));
  mx = max16(mx); mn = min16(mn);
  constexpr float L2E = 1.4426950408889634f;
  const float mxs = -mx * L2E, mns = mn * L2E;
  float ep[8], em[8], sp = 0.f, sm = 0.f;
#pragma unroll
  for (int e = 0; e < 8; e++) {
    const float p = e < 4 ? p0[e & 3] : p1[e & 3];
    ep[e] = __builtin_amdgcn_exp2f(__builtin_fmaf(p, L2E, mxs));
    em[e] = __builtin_amdgcn_exp2f(__builtin_fmaf(p, -L2E, mns));
    sp += ep[e]; sm += em[e];
  }
  sp = __builtin_amdgcn_rcpf(sum16(sp)); sm = __builtin_amdgcn_rcpf(sum16(sm));
#pragma unroll
  for (int e = 0; e < 4; e++) { plus[e] = pk(ep[2 * e] * sp, ep[2 * e + 1] * sp); minus[e] = pk(em[2 * e] * sm, em[2 * e + 1] * sm); }
}
DI v4u pack8(const f32x4& a, const f32x4& b) { return (v4u){pk(a[0], a[1]), pk(a[2], a[3]), pk(b[0], b[1]), pk(b[2], b[3])}; }
DI void wait16(const GAS u64* line, u64 tag, int lane) {
#pragma nounroll
  for (unsigned spins = 0; spins < SPIN_CAP; ++spins) {
    const u64 v = __hip_atomic_load(line + (lane & 15), RLX_AGENT);
    if (__all(v == tag)) break;
    __builtin_amdgcn_s_sleep(1);
  }
  __builtin_amdgcn_fence(__ATOMIC_ACQUIRE, "wavefront");
}
DI void publish(GAS u64* flag, u64 tag, int lane) {
  WAITV(0);
  if (lane == 0) __hip_atomic_store(flag, tag, RLX_AGENT);
}

#define STAMP(i) do {} while (0)

__global__ void __launch_bounds__(512, 2) fused(Params p) {
  __shared__ __attribute__((aligned(16))) char smem[163840];
  LAS char* lds = (LAS char*)smem;
  const int tid = threadIdx.x, lane = tid & 63, w = __builtin_amdgcn_readfirstlane(tid >> 6);
  const int g = lane >> 4, j = lane & 15, q4 = (lane & 15) >> 2, p4 = lane & 3;
  const int tg = w >> 1, ch = w & 1;
  const int b = blockIdx.x, xcd = b & 7, idx = b >> 3;
  const int rt = (xcd >> 2) * 8 + (idx >> 2), c4 = idx & 3;
  const bool isM = c4 >= 2;
  const int grp = (xcd & 3) * 2 + (c4 & 1);
  const int u0 = grp < 3 ? 6 * grp : 18 + 5 * (grp - 3);
  const int rot = idx >> 2;
  const int panel = xcd * 4 + (idx >> 3), ct = idx & 7;
  STAMP(0);
  const unsigned long long tstart = __builtin_amdgcn_s_memrealtime();

  const __amdgpu_buffer_rsrc_t rs_ws = __builtin_amdgcn_make_buffer_rsrc(p.ws, 0, WS_END, 0x00020000);
  const __amdgpu_buffer_rsrc_t rs_x = __builtin_amdgcn_make_buffer_rsrc((void*)p.x, 0, S * D * 2, 0x00020000);
  const __amdgpu_buffer_rsrc_t rs_w = __builtin_amdgcn_make_buffer_rsrc((void*)(isM ? p.w1 : p.wqkv), 0, isM ? D * F * 2 : D * 1536 * 2, 0x00020000);
  const __amdgpu_buffer_rsrc_t rs_w2 = __builtin_amdgcn_make_buffer_rsrc((void*)p.w2, 0, F * D * 2, 0x00020000);
  const __amdgpu_buffer_rsrc_t rs_wo = __builtin_amdgcn_make_buffer_rsrc((void*)p.wo, 0, D * D * 2, 0x00020000);
  GAS char* wsp = (GAS char*)p.ws;

  {
    unsigned long long pc;
    asm volatile("s_getpc_b64 %0" : "=s"(pc));
    pc &= ~255ull;
    if (w < 2) {
      const GAS char* a = (const GAS char*)pc + (w * 32 + idx) * 1024 + lane * 16;
      if (w * 32 + idx < 32) asm volatile("s_mov_b32 m0, %0\n\ts_nop 0\n\tglobal_load_lds_dwordx4 %1, off" ::"s"(2 * w * 1024), "v"(a) : "memory");
    }

  }
  v2u ev;
  asm volatile("global_load_dwordx2 %0, %1, off sc1" : "=v"(ev) : "v"(wsp + WS_E) : "memory");
  v4u aux[16]; v2u b1r[6]; v4u wph;

  unsigned xv[2], wv[3];
#pragma unroll
  for (int i = 0; i < 2; i++) { const int row = 8 * (2 * w + i) + (lane >> 3); xv[i] = (rt * 128 + row) * 1024 + (((lane & 7) ^ ((row >> 1) & 7)) * 16); }
#pragma unroll
  for (int i = 0; i < 3; i++) {
    const int n = (3 * w + i) * 64 + lane, k = n / 24, cp = n % 24;
    const int c = cp ^ (((k >> 1) & 1) | (((k >> 3) & 1) << 2));
    const int seg = c >> 3;
    wv[i] = isM ? k * 2752 + u0 * 64 + c * 16 : k * 3072 + (seg == 2 ? 0 : (seg + 1) * 1024) + grp * 128 + (c & 7) * 16;
  }
  const unsigned wstep = isM ? 64 * 2752 : 64 * 3072;
  const unsigned w2v = (16 * w + (lane >> 3)) * 1024 + ct * 128 + (((lane & 7) ^ ((lane >> 3) & 6)) * 16);
  auto stage = [&](int t) {
    const int kt = (t + rot) & 7;
    LAS char* dst = lds + (t & 3) * SLOT;
    glds(rs_x, dst + (2 * w) * 1024, xv[0], kt * 128);
    glds(rs_x, dst + (2 * w + 1) * 1024, xv[1], kt * 128);
#pragma unroll
    for (int i = 0; i < 3; i++) glds(rs_w, dst + WOFF + (3 * w + i) * 1024, wv[i], kt * wstep);
  };
  auto round_ld = [&](int r, int page) {
    LAS char* dst = lds + page * PAGE + w * 2048;
    if (r < 11) { glds(rs_w2, dst, w2v, r * 131072); glds(rs_w2, dst + 1024, w2v + 8192, r * 131072); }
    else { glds(rs_wo, dst, w2v, (r - 11) * 131072); glds(rs_wo, dst + 1024, w2v + 8192, (r - 11) * 131072); }
  };

  const int xb0 = (32 * tg + j) * 128 + ((g ^ (j >> 1)) * 16);
  const int sk = ((q4 >> 1) & 1) | ((g & 1) << 2);
  const int wrow = WOFF + (8 * g + q4) * 384 + 8 * (p4 & 1);
  const int wb0 = wrow + 16 * ((4 * ch + 2 * (p4 >> 1)) ^ sk), wb1 = wrow + 16 * ((4 * ch + 2 * (p4 >> 1) + 1) ^ sk);
  const int cs1 = tg < 2 ? tg + 4 : tg;
  const int cb0 = ((tg & 1) ? wb1 : wb0) + (tg >> 1) * 128, cb1 = ((cs1 & 1) ? wb1 : wb0) + (cs1 >> 1) * 128;

  f32x4 acc[2][6];
#pragma unroll
  for (int a = 0; a < 2; a++)
#pragma unroll
    for (int t = 0; t < 6; t++) acc[a][t] = (f32x4){0.f, 0.f, 0.f, 0.f};
  f32x4 cs[2] = {{0.f, 0.f, 0.f, 0.f}, {0.f, 0.f, 0.f, 0.f}}, ax = {0.f, 0.f, 0.f, 0.f};
  float ssq = 0.f;
  const v4u ones = {0x3F803F80u, 0x3F803F80u, 0x3F803F80u, 0x3F803F80u};

  auto kloop = [&](auto ism) {
    constexpr bool M = decltype(ism)::value;
    v4u xf[2][2], wf[2][6], cf[2][2];
    if (M) {
      const __amdgpu_buffer_rsrc_t rs_b1 = __builtin_amdgcn_make_buffer_rsrc((void*)p.b1, 0, F * 2, 0x00020000);
#pragma unroll
      for (int t = 0; t < 6; t++) {
        const unsigned off = ((u0 + 2 * (t >> 1) + ch) * 32 + 16 * (g >> 1) + 8 * (t & 1) + 4 * (g & 1)) * 2;
        asm volatile("buffer_load_dwordx2 %0, %1, %2, 0 offen" : "=v"(b1r[t]) : "v"(off), "s"(rs_b1) : "memory");
      }
    } else {
      const GAS char* a = (const GAS char*)p.wphi + (4 * grp + 2 * ch) * 512 + lane * 16;
      asm volatile("global_load_dwordx4 %0, %1, off" : "=v"(wph) : "v"(a) : "memory");
      const __amdgpu_buffer_rsrc_t rs_gw = __builtin_amdgcn_make_buffer_rsrc((void*)p.gw, 0, D * 2, 0x00020000);
#pragma unroll
      for (int s = 0; s < 16; s++)
        asm volatile("buffer_load_dwordx4 %0, %1, %2, %3 offen" : "=v"(aux[s]) : "v"(16 * g), "s"(rs_gw), "s"(((((s >> 1) + rot) & 7) * 64 + (s & 1) * 32) * 2) : "memory");
    }
    stage(0); stage(1); stage(2); stage(3);
    auto rd = [&](int t, int ks) {
      LAS char* sl = lds + (t & 3) * SLOT;
#pragma unroll
      for (int a = 0; a < 2; a++) xf[ks][a] = *(LAS v4u*)(sl + (xb0 ^ (ks * 64)) + a * 2048);
#pragma unroll
      for (int t6 = 0; t6 < 6; t6++) wf[ks][t6] = trr2(sl + ((t6 & 1) ? wb1 : wb0) + (t6 >> 1) * 128 + ks * 12288, 1536);
      if (M) { cf[ks][0] = trr2(sl + cb0 + ks * 12288, 1536); cf[ks][1] = trr2(sl + cb1 + ks * 12288, 1536); }
    };
    auto mm = [&](int ks, int t0, int t1) {
#pragma unroll
      for (int t6 = t0; t6 < t1; t6++)
#pragma unroll
        for (int a = 0; a < 2; a++) acc[a][t6] = mfma(wf[ks][t6], xf[ks][a], acc[a][t6]);
    };
    auto extra = [&](int i, int ks) {
      const v4u xs = ch ? xf[ks][1] : xf[ks][0];
      if (M) {
        cs[0] = mfma(cf[ks][0], ones, cs[0]); cs[1] = mfma(cf[ks][1], ones, cs[1]);
        ax = mfma(ones, xs, ax);
#pragma unroll
        for (int e = 0; e < 4; e++) ssq = __builtin_amdgcn_fdot2_f32_bf16(asb2(xs[e]), asb2(xs[e]), ssq, false);
        asm volatile("" : "+v"(cs[0]), "+v"(cs[1]), "+v"(ssq));
      } else ax = mfma(aux[2 * i + ks], xs, ax);
      asm volatile("" : "+v"(ax));
    };
    STAMP(21);
    WAITV(15);
    asm volatile("" : "+v"(ev));
    if (M) {
#pragma unroll
      for (int t = 0; t < 6; t++) asm volatile("" : "+v"(b1r[t]));
    } else {
      asm volatile("" : "+v"(wph));
#pragma unroll
      for (int s = 0; s < 16; s++) asm volatile("" : "+v"(aux[s]));
    }
    BAR();
    STAMP(13);
    rd(0, 0);
#pragma unroll
    for (int i = 0; i < 8; i++) {
      rd(i, 1);
      __builtin_amdgcn_sched_barrier(0);
      mm(0, 0, 6); extra(i, 0);
      mm(1, 0, 3); extra(i, 1);
      __builtin_amdgcn_sched_barrier(0);
      if (i < 7) {
        if (i < 5) WAITV(10); else if (i == 5) WAITV(9); else WAITV(8);
        LBAR();
        STAMP(14 + i);
        rd(i + 1, 0);
        if (i < 4) stage(i + 4);
        if (i == 4) { round_ld(0, 0); round_ld(1, 1); }
        if (i == 5) { round_ld(2, 2); round_ld(3, 3); }
        if (i == 6 && M) { round_ld(4, 4); round_ld(5, 5); round_ld(6, 6); }
        __builtin_amdgcn_sched_barrier(0);
      }
      mm(1, 3, 6);
    }
  };

  u64 tag;
  v4u phq[2][2];
  if (isM) {
    kloop(std::true_type{});
    STAMP(1);
    { u64 z = (((u64)ev[1] << 32) | ev[0]) + 0x9E3779B97F4A7C15ull; z = (z ^ (z >> 30)) * 0xBF58476D1CE4E5B9ull; z = (z ^ (z >> 27)) * 0x94D049BB133111EBull; tag = z ^ (z >> 31); }
    ssq = sum32(sum16(ssq));
    if (g == 0) {
      const float mean = ax[0] * (1.f / D);
      const float var = fmaxf(ssq * (1.f / D) - mean * mean, 0.f);
      *(LAS f32x2*)(lds + STAT + (w * 16 + j) * 8) = (f32x2){mean, __builtin_amdgcn_rsqf(var + 1e-5f)};
    }
    if (j == 0) {
      *(LAS f32x4*)(lds + STAT + 1024 + ((ch * 6 + tg) * 16 + 4 * g) * 4) = cs[0];
      *(LAS f32x4*)(lds + STAT + 1024 + ((ch * 6 + cs1) * 16 + 4 * g) * 4) = cs[1];
    }
    LBAR();
    round_ld(8, 8); round_ld(9, 9);
    f32x2 st[2];
#pragma unroll
    for (int a = 0; a < 2; a++) st[a] = *(LAS f32x2*)(lds + STAT + ((tg * 2 + a) * 16 + j) * 8);
#pragma unroll
    for (int sgm = 0; sgm < 3; sgm++) {
      f32x4 csv[2];
#pragma unroll
      for (int t2 = 0; t2 < 2; t2++) csv[t2] = *(LAS f32x4*)(lds + STAT + 1024 + ((ch * 6 + sgm * 2 + t2) * 16 + 4 * g) * 4);
      const int lu = 2 * sgm + ch;
#pragma unroll
      for (int a = 0; a < 2; a++) {
        f32x4 hv[2];
#pragma unroll
        for (int t2 = 0; t2 < 2; t2++)
#pragma unroll
          for (int r = 0; r < 4; r++) {
            const unsigned bb = b1r[sgm * 2 + t2][r >> 1];
            const float bias = (r & 1) ? bfhi(bb) : bflo(bb);
            const float v = __builtin_fmaf(__builtin_fmaf(-st[a][0], csv[t2][r], acc[a][sgm * 2 + t2][r]), st[a][1], bias);
            const float u = v * __builtin_fmaf(v * v, -0.044715f * 2.302208198f, -2.302208198f);
            hv[t2][r] = v * __builtin_amdgcn_rcpf(1.f + __builtin_amdgcn_exp2f(u));
          }
        if (lu < 5 || grp < 3)
          __builtin_amdgcn_raw_buffer_store_b128(pack8(hv[0], hv[1]), rs_ws, WS_ACT + (((rt * 4 + tg) * 60 + u0 + lu) * 2 + a) * 1024 + lane * 16, 0, AUX_SC1);
      }
    }
    STAMP(2);
    publish((GAS u64*)(wsp + WS_HF) + ((rt * 4 + tg) * 16 + grp * 2 + ch), tag, lane);
    STAMP(3);
  } else {
    kloop(std::false_type{});
    STAMP(1);
    { u64 z = (((u64)ev[1] << 32) | ev[0]) + 0x9E3779B97F4A7C15ull; z = (z ^ (z >> 30)) * 0xBF58476D1CE4E5B9ull; z = (z ^ (z >> 27)) * 0x94D049BB133111EBull; tag = z ^ (z >> 31); }
    LAS char* mine = lds + IMG + (2 * ch * 128 + 32 * tg) * 96;
    *(LAS v4u*)(mine + lane * 16) = wph;
    v4u wp[2];
    const bool act = (j >> 3) == (g >> 1);
#pragma unroll
    for (int m = 0; m < 2; m++) {
      wp[m] = trr2(mine + ((g >> 1) * 16 + 4 * (g & 1) + q4) * 32 + (8 * m + 4 * (p4 & 1)) * 2, 256);
#pragma unroll
      for (int e = 0; e < 4; e++) wp[m][e] = act ? wp[m][e] : 0u;
    }
    const f32x4 zero = {0.f, 0.f, 0.f, 0.f};
#pragma unroll
    for (int a = 0; a < 2; a++) {
      const v4u kf = pack8(acc[a][0], acc[a][1]);
      v4u pl, mi;
      dual_softmax(mfma(wp[0], kf, zero), mfma(wp[1], kf, zero), pl, mi);
      LAS char* row = lds + IMG + ((2 * ch + (g >> 1)) * 128 + 32 * tg + 16 * a + j) * 96;
      *(LAS v4u*)(row + 16 * (g & 1)) = pl;
      *(LAS v4u*)(row + 32 + 16 * (g & 1)) = mi;
      *(LAS v2u*)(row + 64 + 8 * (g & 1)) = (v2u){pk(acc[a][2][0], acc[a][2][1]), pk(acc[a][2][2], acc[a][2][3])};
      *(LAS v2u*)(row + 80 + 8 * (g & 1)) = (v2u){pk(acc[a][3][0], acc[a][3][1]), pk(acc[a][3][2], acc[a][3][3])};
    }
    LBAR();
    f32x4 kv = zero, zz = zero;
    LAS char* ib = lds + IMG + ((w >> 1) * 128 + 4 * g + q4) * 96 + 8 * p4;
#pragma unroll
    for (int ks = 0; ks < 4; ks++) {
      const v4u pa = trr2(ib + ks * 32 * 96 + 32 * (w & 1), 16 * 96), vb = trr2(ib + ks * 32 * 96 + 64, 16 * 96);
      kv = mfma(pa, vb, kv); zz = mfma(pa, ones, zz);
    }
    __builtin_amdgcn_raw_buffer_store_b128(__builtin_bit_cast(v4u, kv), rs_ws, WS_KVP + ((grp * 16 + rt) * 8 + w) * 1024 + lane * 16, 0, AUX_SC1);
    if (j == 0) __builtin_amdgcn_raw_buffer_store_b128(__builtin_bit_cast(v4u, zz), rs_ws, WS_ZP + ((grp * 8 + w) * 16 + rt) * 64 + g * 16, 0, AUX_SC1);
    STAMP(2);
    publish((GAS u64*)(wsp + WS_KVF) + ((grp * 8 + w) * 16 + rt), tag, lane);
    STAMP(3);
#pragma unroll
    for (int a = 0; a < 2; a++) {
      const v4u qf = pack8(acc[a][4], acc[a][5]);
      dual_softmax(mfma(wp[0], qf, zero), mfma(wp[1], qf, zero), phq[a][0], phq[a][1]);
    }
    if (g == 0 && grp == 0) {
      const float gate = __builtin_amdgcn_rcpf(1.f + __builtin_amdgcn_exp2f(-1.4426950408889634f * ax[0]));
      __builtin_amdgcn_raw_buffer_store_b32(__builtin_bit_cast(unsigned, gate), rs_ws, WS_G + (rt * 128 + w * 16 + j) * 4, 0, AUX_SC1);
    }
  }

  const int th = w >> 2, q = w & 3;
  int ab[4];
  { const int kk = 16 * (g >> 1) + 4 * (g & 1) + q4, u6 = kk & 6;
#pragma unroll
    for (int c = 0; c < 4; c++) ab[c] = q * 4096 + kk * 128 + (((2 * c) ^ u6) + (p4 >> 1)) * 16 + 8 * (p4 & 1); }
  BAR();
  v4u wt[4][4];
#pragma unroll
  for (int r = 0; r < 4; r++)
#pragma unroll
    for (int c = 0; c < 4; c++) wt[r][c] = trr2(lds + r * PAGE + ab[c], 1024);
  LBAR();
  if (isM) round_ld(7, 7);
  else { round_ld(4, 4); round_ld(5, 5); round_ld(6, 6); round_ld(7, 7); round_ld(8, 8); }
  round_ld(10, 0); round_ld(11, 1); round_ld(12, 2); round_ld(13, 3);
  STAMP(4);

  if (!isM) {
    wait16((const GAS u64*)(wsp + WS_KVF) + (grp * 8 + w) * 16, tag, lane);
    STAMP(5);
    v4u pt[16];
#pragma unroll
    for (int r = 0; r < 16; r++) pt[r] = __builtin_amdgcn_raw_buffer_load_b128(rs_ws, WS_KVP + (grp * 16 * 8 + w) * 1024 + lane * 16, r * 8192, AUX_SC1);
    const v4u zr = __builtin_amdgcn_raw_buffer_load_b128(rs_ws, WS_ZP + (grp * 8 + w) * 1024 + lane * 16, 0, AUX_SC1);
    __builtin_amdgcn_sched_barrier(0);
    f32x4 kv = __builtin_bit_cast(f32x4, pt[0]);
#pragma unroll
    for (int r = 1; r < 16; r++) kv += __builtin_bit_cast(f32x4, pt[r]);
    *(LAS v2u*)(lds + KVR + ((w >> 1) * 17 + j) * 64 + (16 * (w & 1) + 4 * g) * 2) = (v2u){pk(kv[0], kv[1]), pk(kv[2], kv[3])};
    f32x4 z = __builtin_bit_cast(f32x4, zr);
#pragma unroll
    for (int e = 0; e < 4; e++) {
      float v = z[e];
      v += __builtin_bit_cast(float, __builtin_amdgcn_update_dpp(0u, __builtin_bit_cast(unsigned, v), 0x124, 0xf, 0xf, false));
      v += __builtin_bit_cast(float, __builtin_amdgcn_update_dpp(0u, __builtin_bit_cast(unsigned, v), 0x128, 0xf, 0xf, false));
      z[e] = sum32(sum16(v));
    }
    if (lane < 4) *(LAS v2u*)(lds + KVR + ((w >> 1) * 17 + 16) * 64 + (16 * (w & 1) + 4 * lane) * 2) = (v2u){pk(z[0], z[1]), pk(z[2], z[3])};
    if (tid == 0) *(LAS v4u*)(lds + KVR_ZERO) = (v4u){0u, 0u, 0u, 0u};
    LBAR();
    STAMP(6);
    const bool act = (j >> 3) == (g >> 1);
    const int hb = KVR + (2 * ch + (g >> 1)) * 17 * 64 + 16 * (g & 1);
    v4u ka[2][2], za[2];
#pragma unroll
    for (int sg = 0; sg < 2; sg++) {
#pragma unroll
      for (int dh = 0; dh < 2; dh++) ka[sg][dh] = *(LAS v4u*)(lds + (act ? hb + (8 * dh + (j & 7)) * 64 + 32 * sg : KVR_ZERO));
      za[sg] = *(LAS v4u*)(lds + (act ? hb + 16 * 64 + 32 * sg : KVR_ZERO));
    }
    const f32x4 zero = {0.f, 0.f, 0.f, 0.f};
#pragma unroll
    for (int a = 0; a < 2; a++) {
      f32x4 n0 = mfma(ka[1][0], phq[a][1], mfma(ka[0][0], phq[a][0], zero));
      f32x4 n1 = mfma(ka[1][1], phq[a][1], mfma(ka[0][1], phq[a][0], zero));
      const f32x4 dn = mfma(za[1], phq[a][1], mfma(za[0], phq[a][0], zero));
      const float rd = 1.f / (dn[0] + 1e-6f);
      n0 *= rd; n1 *= rd;
      __builtin_amdgcn_raw_buffer_store_b128(pack8(n0, n1), rs_ws, WS_ACT + (((rt * 4 + tg) * 60 + 44 + 2 * grp + ch) * 2 + a) * 1024 + lane * 16, 0, AUX_SC1);
    }
    publish((GAS u64*)(wsp + WS_OF) + ((rt * 4 + tg) * 16 + grp * 2 + ch), tag, lane);
    STAMP(7);
    BAR();
    round_ld(9, 9);
  }

  const int tgi = panel * 2 + th;
  wait16((const GAS u64*)(wsp + WS_HF) + tgi * 16, tag, lane);
  STAMP(8);
  const unsigned av = WS_ACT + (tgi * 60 + q) * 2048 + lane * 16;
  v4u bf[11][2];
#pragma unroll
  for (int r = 0; r < 11; r++)
#pragma unroll
    for (int a = 0; a < 2; a++)
      bf[r][a] = __builtin_amdgcn_raw_buffer_load_b128(rs_ws, av + a * 1024, (r == 10 && q == 3) ? WS_END : r * 8192, AUX_SC1);
  v2u b2r[4];
#pragma unroll
  for (int c = 0; c < 4; c++) b2r[c] = *(const GAS v2u*)((const GAS char*)p.b2 + (ct * 64 + 16 * c + 4 * g) * 2);
  __builtin_amdgcn_sched_barrier(0);
  f32x4 ob[2][4], oa[2][4];
#pragma unroll
  for (int a = 0; a < 2; a++)
#pragma unroll
    for (int c = 0; c < 4; c++) { ob[a][c] = (f32x4){0.f, 0.f, 0.f, 0.f}; oa[a][c] = (f32x4){0.f, 0.f, 0.f, 0.f}; }
#pragma unroll
  for (int r = 0; r < 11; r++) {
    if (r == 7) BAR();
#pragma unroll
    for (int c = 0; c < 4; c++) {
      const v4u wf = r < 4 ? wt[r < 4 ? r : 0][c] : trr2(lds + (r < 10 ? r : 0) * PAGE + ab[c], 1024);
#pragma unroll
      for (int a = 0; a < 2; a++) ob[a][c] = mfma(wf, bf[r][a], ob[a][c]);
    }
  }
  STAMP(9);
  BAR();
  {
    const unsigned pv = (32 * q + (lane >> 3)) * 1024 + ct * 128 + (((lane & 7) ^ ((lane >> 3) & 6)) * 16);
#pragma unroll
    for (int i = 0; i < 4; i++) glds(rs_wo, lds + 4 * PAGE + w * 4096 + i * 1024, pv + i * 8192, 3 * 131072);
  }
  if (b == 0 && w == 7) {
#pragma nounroll
    for (unsigned spins = 0; spins < SPIN_CAP; ++spins) {
      bool ok = true;
#pragma unroll
      for (int i = 0; i < 16; i++) ok &= __hip_atomic_load((const GAS u64*)(wsp + WS_HF) + i * 64 + lane, RLX_AGENT) == tag;
      if (__all(ok)) break;
      __builtin_amdgcn_s_sleep(1);
    }
  }
  wait16((const GAS u64*)(wsp + WS_OF) + tgi * 16, tag, lane);
  STAMP(10);
  if (b == 0 && w == 7 && lane == 0) __hip_atomic_store((GAS u64*)(wsp + WS_E), tag, RLX_AGENT);
  float gate[2];
  v4u of[4][2];
#pragma unroll
  for (int r = 0; r < 4; r++)
#pragma unroll
    for (int a = 0; a < 2; a++) of[r][a] = __builtin_amdgcn_raw_buffer_load_b128(rs_ws, av + a * 1024, (11 + r) * 8192, AUX_SC1);
#pragma unroll
  for (int a = 0; a < 2; a++) gate[a] = __builtin_bit_cast(float, __builtin_amdgcn_raw_buffer_load_b32(rs_ws, WS_G + (tgi * 32 + 16 * a + j) * 4, 0, AUX_SC1));
  __builtin_amdgcn_sched_barrier(0);
#pragma unroll
  for (int r = 0; r < 4; r++)
#pragma unroll
    for (int c = 0; c < 4; c++) {
      const v4u wf = trr2(lds + (r < 3 ? (r + 1) * PAGE + ab[c] : 4 * PAGE + (w - q) * 4096 + ab[c]), 1024);
#pragma unroll
      for (int a = 0; a < 2; a++) oa[a][c] = mfma(wf, of[r][a], oa[a][c]);
    }
  STAMP(11);
#pragma unroll
  for (int a = 0; a < 2; a++)
#pragma unroll
    for (int c = 0; c < 4; c++) {
      f32x4 v;
#pragma unroll
      for (int r = 0; r < 4; r++) {
        const unsigned bb = b2r[c][r >> 1];
        const float bias = q == 0 ? ((r & 1) ? bfhi(bb) : bflo(bb)) : 0.f;
        v[r] = gate[a] * oa[a][c][r] + (1.f - gate[a]) * (ob[a][c][r] + bias);
      }
      *(LAS f32x4*)(lds + RED + (q * 64 + 32 * th + 16 * a + j) * 256 + (((4 * c + g) ^ (j & 7)) * 16)) = v;
    }
  LBAR();
  {
    const int tok = tid >> 3, c8 = tid & 7;
    f32x4 s0 = {0.f, 0.f, 0.f, 0.f}, s1 = {0.f, 0.f, 0.f, 0.f};
#pragma unroll
    for (int qq = 0; qq < 4; qq++) {
      s0 += *(LAS f32x4*)(lds + RED + (qq * 64 + tok) * 256 + (((2 * c8) ^ (tok & 7)) * 16));
      s1 += *(LAS f32x4*)(lds + RED + (qq * 64 + tok) * 256 + (((2 * c8 + 1) ^ (tok & 7)) * 16));
    }
    *(GAS v4u*)((GAS char*)p.out + ((panel * 64 + tok) * 512 + ct * 64 + c8 * 8) * 2) = pack8(s0, s1);
  }
  STAMP(12);
  while (__builtin_amdgcn_s_memrealtime() - tstart < 6000ull) __builtin_amdgcn_s_sleep(1);
}
}

extern "C" void kernel_launch(void* const* d_in, const int* in_sizes, int n_in, void* d_out, int out_size, void* d_ws, size_t ws_size,
                              hipStream_t stream) {
  gm::Params p;
  p.x = d_in[0]; p.wqkv = d_in[1]; p.wphi = d_in[2]; p.wo = d_in[3];
  p.w1 = d_in[5]; p.b1 = d_in[6]; p.w2 = d_in[7]; p.b2 = d_in[8]; p.gw = d_in[9];
  p.out = d_out; p.ws = d_ws;
  gm::fused<<<256, 512, 0, stream>>>(p);
}
```

```cpp
#include <hip/hip_runtime.h>
#include <hip/hip_bf16.h>

typedef unsigned long long u64;
typedef unsigned v4u __attribute__((ext_vector_type(4)));
typedef unsigned v2u __attribute__((ext_vector_type(2)));
typedef float f32x16 __attribute__((ext_vector_type(16)));
typedef float f32x4 __attribute__((ext_vector_type(4)));
typedef float f32x2 __attribute__((ext_vector_type(2)));
typedef __bf16 bf16x8 __attribute__((ext_vector_type(8)));
typedef __bf16 bf16x2 __attribute__((ext_vector_type(2)));
typedef short s16x4 __attribute__((ext_vector_type(4)));
#define GAS __attribute__((address_space(1)))
#define LAS __attribute__((address_space(3)))
#define DI __device__ __forceinline__
#define RLX_AGENT __ATOMIC_RELAXED, __HIP_MEMORY_SCOPE_AGENT

#define STAMPS 0
#define CODE_KB 12

namespace gm {
constexpr int S = 2048, D = 512, F = 1376;
constexpr unsigned WS_E = 0, WS_KVF = 4096, WS_HF = 12288, WS_OF = 20480, WS_ZP = 40960, WS_KVP = 131072, WS_ST = 1310720,
                   WS_ACT = 2u << 20, ACT_BYTES = 16u * 4 * 60 * 2048, WS_END = WS_ACT + ACT_BYTES, WS_STAMP = 16u << 20;
constexpr int SLOT = 40960, WOFF = 16384, PAGE = 16384;
constexpr int IMG = 7 * PAGE;
constexpr int KVR = 3 * PAGE;
constexpr int KVR_ZERO = KVR + 4 * 17 * 64;
constexpr int STAT = 7 * PAGE;
constexpr int RED = 6 * PAGE;
constexpr int AUX_SC1 = 16;
constexpr unsigned SPIN_CAP = 1u << 16;

struct Params {
  const void *x, *wqkv, *wphi, *wo, *w1, *b1, *w2, *b2, *gw;
  void *out, *ws;
};

DI float asf(unsigned u) { return __builtin_bit_cast(float, u); }
DI unsigned asu(float f) { return __builtin_bit_cast(unsigned, f); }
DI bf16x2 asb2(unsigned u) { return __builtin_bit_cast(bf16x2, u); }
DI f32x4 mfma16(v4u a, v4u b, f32x4 c) {
  return __builtin_amdgcn_mfma_f32_16x16x32_bf16(__builtin_bit_cast(bf16x8, a), __builtin_bit_cast(bf16x8, b), c, 0, 0, 0);
}
DI f32x16 mfma32(v4u a, v4u b, f32x16 c) {
  return __builtin_amdgcn_mfma_f32_32x32x16_bf16(__builtin_bit_cast(bf16x8, a), __builtin_bit_cast(bf16x8, b), c, 0, 0, 0);
}
DI unsigned pk(float a, float b) { return __builtin_bit_cast(unsigned, __builtin_convertvector((f32x2){a, b}, bf16x2)); }
DI unsigned pk2(f32x2 v) { return __builtin_bit_cast(unsigned, __builtin_convertvector(v, bf16x2)); }
DI float bflo(unsigned u) { return __builtin_bit_cast(float, u << 16); }
DI float bfhi(unsigned u) { return __builtin_bit_cast(float, u & 0xffff0000u); }
DI v2u trr(LAS char* p) { return __builtin_bit_cast(v2u, __builtin_amdgcn_ds_read_tr16_b64_v4i16((LAS s16x4*)p)); }
DI v4u trr2(LAS char* p, int off2) { v2u a = trr(p), b = trr(p + off2); return (v4u){a[0], a[1], b[0], b[1]}; }
DI f32x2 pair16(float v) { auto r = __builtin_amdgcn_permlane16_swap(asu(v), asu(v), false, false); return (f32x2){asf(r[0]), asf(r[1])}; }
DI f32x2 pair32(float v) { auto r = __builtin_amdgcn_permlane32_swap(asu(v), asu(v), false, false); return (f32x2){asf(r[0]), asf(r[1])}; }
DI float sum16(float v) { const f32x2 r = pair16(v); return r[0] + r[1]; }
DI float sum32(float v) { const f32x2 r = pair32(v); return r[0] + r[1]; }
DI f32x2 fma2(f32x2 a, f32x2 b, f32x2 c) { return __builtin_elementwise_fma(a, b, c); }
DI f32x2 bc2(float v) { return (f32x2){v, v}; }
#define WAITV(n) asm volatile("s_waitcnt vmcnt(%0)" ::"n"(n) : "memory")
#define WAITL() asm volatile("s_waitcnt lgkmcnt(0)" ::: "memory")
#define LBAR() do { WAITL(); __builtin_amdgcn_s_barrier(); asm volatile("" ::: "memory"); } while (0)
#define SB() __builtin_amdgcn_sched_barrier(0)

DI void glds(__amdgpu_buffer_rsrc_t rs, unsigned dst, unsigned voff, unsigned soff) {
  asm volatile("s_mov_b32 m0, %0\n\ts_nop 0\n\tbuffer_load_dwordx4 %1, %2, %3 offen lds" ::"s"(__builtin_amdgcn_readfirstlane(dst)), "v"(voff), "s"(rs), "s"(__builtin_amdgcn_readfirstlane(soff)) : "memory");
}
DI v4u pack8(const f32x16& a, int o) { return (v4u){pk(a[o], a[o + 1]), pk(a[o + 2], a[o + 3]), pk(a[o + 4], a[o + 5]), pk(a[o + 6], a[o + 7])}; }
DI void dual_softmax(const f32x16& p, v4u (&o)[4]) {
  float mx = fmaxf(fmaxf(p[0], p[1]), p[2]), mn = fminf(fminf(p[0], p[1]), p[2]);
#pragma unroll
  for (int e = 3; e < 15; e += 2) { mx = fmaxf(fmaxf(mx, p[e]), p[e + 1]); mn = fminf(fminf(mn, p[e]), p[e + 1]); }
  mx = fmaxf(fmaxf(mx, p[15]), p[14]); mn = fminf(fminf(mn, p[15]), p[14]);
  constexpr float L2E = 1.4426950408889634f;
  const float mxs = -mx * L2E, mns = mn * L2E;
  f32x2 ep[8], em[8];
#pragma unroll
  for (int e = 0; e < 8; e++) {
    const f32x2 pp = {p[2 * e], p[2 * e + 1]};
    const f32x2 a = fma2(pp, bc2(L2E), bc2(mxs)), c = fma2(pp, bc2(-L2E), bc2(mns));
    ep[e] = (f32x2){__builtin_amdgcn_exp2f(a[0]), __builtin_amdgcn_exp2f(a[1])};
    em[e] = (f32x2){__builtin_amdgcn_exp2f(c[0]), __builtin_amdgcn_exp2f(c[1])};
  }
  const f32x2 sp = ((ep[0] + ep[1]) + (ep[2] + ep[3])) + ((ep[4] + ep[5]) + (ep[6] + ep[7]));
  const f32x2 sm = ((em[0] + em[1]) + (em[2] + em[3])) + ((em[4] + em[5]) + (em[6] + em[7]));
  const float rp = __builtin_amdgcn_rcpf(sp[0] + sp[1]), rm = __builtin_amdgcn_rcpf(sm[0] + sm[1]);
#pragma unroll
  for (int e = 0; e < 8; e++) { o[e >> 2][e & 3] = pk2(ep[e] * rp); o[2 + (e >> 2)][e & 3] = pk2(em[e] * rm); }
}
DI void wait16(const GAS u64* line, u64 tag, int lane) {
#pragma nounroll
  for (unsigned spins = 0; spins < SPIN_CAP; ++spins) {
    const u64 v = __hip_atomic_load(line + (lane & 15), RLX_AGENT);
    if (__all(v == tag)) break;
    __builtin_amdgcn_s_sleep(1);
  }
  __builtin_amdgcn_fence(__ATOMIC_ACQUIRE, "wavefront");
}
DI void publish(GAS u64* flag, u64 tag, int lane) {
  WAITV(0);
  if (lane == 0) __hip_atomic_store(flag, tag, RLX_AGENT);
}

#define STAMP(i) do {} while (0)

__global__ void __launch_bounds__(512, 2) fused(Params p) {
  __shared__ __attribute__((aligned(16))) char smem[163840];
  LAS char* lds = (LAS char*)smem;
  const int tid = threadIdx.x, lane = tid & 63, w = __builtin_amdgcn_readfirstlane(tid >> 6);
  const int h = lane >> 5, j32 = lane & 31, blk = (lane >> 4) & 1, g = lane >> 4, j = lane & 15, q4 = (lane & 15) >> 2, p4 = lane & 3;
  const int tg = w >> 1, ch = w & 1;
  const int b = blockIdx.x, xcd = b & 7, idx = b >> 3;
  const int rt = (xcd >> 2) * 8 + (idx >> 2), c4 = idx & 3;
  const bool isM = c4 >= 2;
  const int grp = (xcd & 3) * 2 + (c4 & 1);
  const int u0 = grp < 3 ? 6 * grp : 3 + 5 * grp;
  const int rot = idx >> 2;
  const int panel = xcd * 4 + (idx >> 3), ct = idx & 7;
  STAMP(0);
  GAS char* wsp = (GAS char*)p.ws;
  const __amdgpu_buffer_rsrc_t rs_x = __builtin_amdgcn_make_buffer_rsrc((void*)p.x, 0, S * D * 2, 0x00020000);
  const __amdgpu_buffer_rsrc_t rs_w = __builtin_amdgcn_make_buffer_rsrc((void*)(isM ? p.w1 : p.wqkv), 0, isM ? D * F * 2 : D * 1536 * 2, 0x00020000);

  if (w == 0 && idx < CODE_KB) {
    u64 pc;
    asm volatile("s_getpc_b64 %0" : "=s"(pc));
    asm volatile("s_mov_b32 m0, 0\n\ts_nop 0\n\tglobal_load_lds_dwordx4 %0, off" ::"v"((const GAS char*)(pc & ~255ull) + idx * 1024 + lane * 16) : "memory");
  }
  v4u e0, e1, xr; v2u ev;
  const int stok = rt * 128 + 32 * tg + 2 * (grp + (isM ? 8 : 0)) + ch;
  {
    const int t = j < 6 ? j : 5, un = (t >= 4 && ch && grp >= 3) ? 0 : (t & 6) + ch;
    const unsigned off = isM ? ((u0 + un) * 32 + 16 * h + 8 * (t & 1)) * 2 : (4 * grp + 2 * ch) * 512 + lane * 16;
    asm volatile("global_load_dwordx4 %0, %1, %2" : "=v"(xr) : "v"(stok * 1024 + lane * 16), "s"(p.x) : "memory");
    asm volatile("global_load_dwordx4 %0, %1, %2" : "=v"(e1) : "v"(lane * 16), "s"(p.gw) : "memory");
    asm volatile("global_load_dwordx2 %0, %1, %2 sc1" : "=v"(ev) : "v"(0), "s"(p.ws) : "memory");
    asm volatile("global_load_dwordx4 %0, %1, %2" : "=v"(e0) : "v"(off), "s"(isM ? p.b1 : p.wphi) : "memory");
  }
  unsigned va[5];
#pragma unroll
  for (int i = 0; i < 2; i++) { const int row = 8 * (2 * w + i) + (lane >> 3); va[i] = (rt * 128 + row) * 1024 + (((lane & 7) ^ ((row >> 1) & 7)) * 16); }
  glds(rs_x, w * 2048, va[0], rot * 128);
  glds(rs_x, w * 2048 + 1024, va[1], rot * 128);
  const unsigned wstr = isM ? 2752 : 3072, segs = isM ? 128 : 1024, wbase = isM ? u0 * 64 : grp * 128;
#pragma unroll
  for (int i = 0; i < 3; i++) {
    const unsigned n = (3 * w + i) * 64 + lane, k = n / 24, cp = n - k * 24;
    const unsigned c = cp ^ ((k & 2) << 1), seg = c >> 3;
    const unsigned sg = isM ? seg : (seg == 2 ? 0 : seg + 1);
    va[2 + i] = k * wstr + sg * segs + wbase + (c & 7) * 16;
  }
  const unsigned wstep = 64 * wstr;
  auto piece = [&](int t, int n) {
    const int kt = (t + rot) & 7;
    if (n < 2) glds(rs_x, t * SLOT + w * 2048 + n * 1024, va[n], kt * 128);
    else glds(rs_w, t * SLOT + WOFF + w * 3072 + (n - 2) * 1024, va[n], kt * wstep);
  };
  piece(0, 2); piece(0, 3); piece(0, 4);
#pragma unroll
  for (int n = 0; n < 5; n++) piece(1, n);

  const unsigned w2v = (8 * w + (lane >> 3)) * 1024 + ct * 128 + (((lane & 7) ^ ((lane >> 2) & 4)) * 16);
  const int xb0 = (32 * tg + j32) * 128 + ((h ^ ((j32 >> 1) & 7)) * 16);
  const int P8 = 4 * blk + p4;
  const int wb0 = WOFF + (8 * h + q4) * 384 + ((ch * 64 + 32 * (P8 & 1) + 8 * (P8 >> 1)) ^ ((q4 & 2) << 5));
  const int cs1 = tg < 2 ? tg + 4 : tg;
  const int crow = WOFF + (8 * g + q4) * 384;
  const int cb0 = crow + (tg >> 1) * 128 + ((ch * 64 + 32 * (tg & 1) + 8 * p4) ^ ((q4 & 2) << 5));
  const int cb1 = crow + (cs1 >> 1) * 128 + ((ch * 64 + 32 * (cs1 & 1) + 8 * p4) ^ ((q4 & 2) << 5));
  const bool act = ((lane >> 2) & 1) == h;
  const __amdgpu_buffer_rsrc_t rs_ws = __builtin_amdgcn_make_buffer_rsrc(p.ws, 0, WS_END, 0x00020000);

  f32x16 zero16;
#pragma unroll
  for (int e = 0; e < 16; e++) zero16[e] = 0.f;
  const f32x4 zero = {0.f, 0.f, 0.f, 0.f};
  const v4u ones = {0x3F803F80u, 0x3F803F80u, 0x3F803F80u, 0x3F803F80u}, z4 = {0u, 0u, 0u, 0u};
  f32x16 acc[3];
  f32x4 cs[2] = {zero, zero};
  v4u wp[2];
  STAMP(21);
  asm volatile("s_waitcnt vmcnt(10)" : "+v"(e0), "+v"(e1), "+v"(xr), "+v"(ev) :: "memory");
  u64 tag;
  { u64 z = (((u64)(unsigned)__builtin_amdgcn_readfirstlane(ev[1]) << 32) | (unsigned)__builtin_amdgcn_readfirstlane(ev[0])) + 0x9E3779B97F4A7C15ull;
    z = (z ^ (z >> 30)) * 0xBF58476D1CE4E5B9ull; z = (z ^ (z >> 27)) * 0x94D049BB133111EBull; tag = z ^ (z >> 31); }
  {
    LAS char* mine = lds + 3 * SLOT + w * 1024;
    *(LAS v4u*)(mine + lane * 16) = e0;
#pragma unroll
    for (int s = 0; s < 2; s++) wp[s] = trr2(mine + (h * 16 + 8 * s + q4) * 32 + 8 * (P8 >> 1), 128);
    float sv[3] = {0.f, 0.f, 0.f};
#pragma unroll
    for (int e = 0; e < 4; e++) {
      sv[0] = __builtin_amdgcn_fdot2_f32_bf16(asb2(xr[e]), asb2(0x3F803F80u), sv[0], false);
      sv[1] = __builtin_amdgcn_fdot2_f32_bf16(asb2(xr[e]), asb2(xr[e]), sv[1], false);
      sv[2] = __builtin_amdgcn_fdot2_f32_bf16(asb2(xr[e]), asb2(e1[e]), sv[2], false);
    }
#pragma unroll
    for (int e = 0; e < 3; e++) {
      float v = sv[e];
      v += asf(__builtin_amdgcn_update_dpp(0u, asu(v), 0x121, 0xf, 0xf, false));
      v += asf(__builtin_amdgcn_update_dpp(0u, asu(v), 0x122, 0xf, 0xf, false));
      v += asf(__builtin_amdgcn_update_dpp(0u, asu(v), 0x124, 0xf, 0xf, false));
      v += asf(__builtin_amdgcn_update_dpp(0u, asu(v), 0x128, 0xf, 0xf, false));
      sv[e] = sum32(sum16(v));
    }
    const float mean = sv[0] * (1.f / D);
    const float rstd = __builtin_amdgcn_rsqf(fmaxf(sv[1] * (1.f / D) - mean * mean, 0.f) + 1e-5f);
    const float gate = __builtin_amdgcn_rcpf(1.f + __builtin_amdgcn_exp2f(-1.4426950408889634f * sv[2]));
    const float val = lane == 0 ? rstd : lane == 1 ? -mean * rstd : gate;
    __builtin_amdgcn_raw_buffer_store_b64((v2u){asu(val), (unsigned)tag}, rs_ws, lane < 3 ? WS_ST + stok * 32 + lane * 8 : WS_END, 0, AUX_SC1);
#pragma unroll
    for (int s = 0; s < 2; s++) {
#pragma unroll
      for (int e = 0; e < 4; e++) wp[s][e] = act ? wp[s][e] : 0u;
      asm volatile("" : "+v"(wp[s]));
    }
  }

  const __amdgpu_buffer_rsrc_t rs_w2 = __builtin_amdgcn_make_buffer_rsrc((void*)p.w2, 0, F * D * 2, 0x00020000);
  auto kloop = [&](auto ism) {
    constexpr bool M = decltype(ism)::value;
    v4u cf[2][2] = {{z4, z4}, {z4, z4}};
    v4u xf[4], wf[4][3];
    xf[2] = z4; xf[3] = z4;
#pragma unroll
    for (int t = 0; t < 3; t++) { acc[t] = zero16; wf[2][t] = z4; wf[3][t] = z4; asm volatile("" : "+v"(acc[t]), "+v"(wf[2][t]), "+v"(wf[3][t])); piece(2, t); }
    asm volatile("" : "+v"(xf[2]), "+v"(xf[3]), "+v"(cf[1][0]), "+v"(cf[1][1]));
    piece(2, 3); piece(2, 4);
    __amdgpu_buffer_rsrc_t ra = rs_x, rb = rs_w;
    unsigned oA = w * 2048, oB = WOFF + w * 3072, dst = 1024, sst = 0, mulB = wstep / 128, addB = 0, dsl = 3 * SLOT;
#pragma nounroll
    for (int i = 0; i < 8; i++) {
      const unsigned so = (i & 3) * SLOT;
      LAS char* wq = lds + so + wb0;
      if (i == 5) {
#pragma unroll
        for (int n = 0; n < 5; n++) va[n] = w2v;
        ra = rs_w2; rb = rs_w2; oA = w * 1024; oB = oA + 16384; dst = 8192; sst = 65536; mulB = 1; addB = 131072;
      }
      WAITV(10);
      LBAR();
      auto rx = [&](int s) { xf[s] = *(LAS v4u*)(lds + ((so + xb0) ^ (32 * s))); };
      auto rw = [&](int s, int t) { wf[s][t] = trr2(wq + t * 128 + s * 6144, 1536); };
      auto mm = [&](int s, int t) { acc[t] = mfma32(wf[s][t], xf[s], acc[t]); SB(); };
      auto rc = [&](int ks) { if (M) { cf[ks][0] = trr2(lds + so + cb0 + ks * 12288, 1536); cf[ks][1] = trr2(lds + so + cb1 + ks * 12288, 1536); } };
      auto mc = [&](int ks) { if (M) { cs[0] = mfma16(cf[ks][0], ones, cs[0]); cs[1] = mfma16(cf[ks][1], ones, cs[1]); SB(); } };
      mm(2, 0); rx(0); rw(0, 0);
      const unsigned sA = i >= 5 ? dsl * 8 : ((i + 3 + rot) & 7) * 128, sB = sA * mulB + addB, dA = dsl + oA, dB = dsl + oB;
      dsl = so;
      if (M) { if (i == 7) asm volatile("s_mov_b32 m0, %0\n\ts_nop 0\n\tbuffer_load_dwordx4 %1, %2, %3 offen sc1 lds" ::"s"(STAT + tg * 1024), "v"(lane * 16), "s"(rs_ws), "s"(WS_ST + (rt * 128 + tg * 32) * 32) : "memory"); }
      SB();
      mm(2, 1); rw(0, 1); rw(0, 2); glds(ra, dA, va[0], sA); SB();
      mm(2, 2); rx(1); rw(1, 0); SB();
      mc(1);
      mm(3, 0); rw(1, 1); rw(1, 2); glds(ra, dA + dst, va[1], sA + sst); SB();
      mm(3, 1); rx(2); rw(2, 0); SB();
      mm(3, 2); rw(2, 1); rw(2, 2); glds(rb, dB, va[2], sB); SB();
      mm(0, 0); rx(3); rw(3, 0); SB();
      mm(0, 1); rw(3, 1); rw(3, 2); glds(rb, dB + dst, va[3], sB + sst); SB();
      mm(0, 2); rc(0); SB();
      mm(1, 0); rc(1); if (i < 7) glds(rb, dB + 2 * dst, va[4], sB + 2 * sst); SB();
      mm(1, 1);
      mc(0);
      mm(1, 2);
    }
    if (M) { cs[0] = mfma16(cf[1][0], ones, cs[0]); cs[1] = mfma16(cf[1][1], ones, cs[1]); }
#pragma unroll
    for (int s = 2; s < 4; s++)
#pragma unroll
      for (int t = 0; t < 3; t++) acc[t] = mfma32(wf[s][t], xf[s], acc[t]);
  };

  const __amdgpu_buffer_rsrc_t rs_wo = __builtin_amdgcn_make_buffer_rsrc((void*)p.wo, 0, D * D * 2, 0x00020000);
  auto rpiece = [&](int r, int page, int n) {
    if (r < 11) glds(rs_w2, page * PAGE + n * 8192 + w * 1024, w2v, r * 131072 + n * 65536);
    else glds(rs_wo, page * PAGE + n * 8192 + w * 1024, w2v, (r - 11) * 131072 + n * 65536);
  };
  auto round_ld = [&](int r, int page) { rpiece(r, page, 0); rpiece(r, page, 1); };
  const unsigned actw = WS_ACT + (rt * 4 + tg) * 60 * 2048 + lane * 16;

  if (isM) {
    kloop(std::true_type{});
    STAMP(1);
    if (j == 0) {
      *(LAS f32x4*)(lds + STAT + 4096 + (ch * 96 + tg * 16 + 4 * g) * 4) = cs[0];
      *(LAS f32x4*)(lds + STAT + 4096 + (ch * 96 + cs1 * 16 + 4 * g) * 4) = cs[1];
    }
    if (j < 6 && blk == 0 && tg == 0) {
      LAS char* d = lds + STAT + 5120 + (ch * 96 + (j >> 1) * 32 + 16 * h + 8 * (j & 1)) * 4;
      *(LAS f32x4*)d = (f32x4){bflo(e0[0]), bfhi(e0[0]), bflo(e0[1]), bfhi(e0[1])};
      *(LAS f32x4*)(d + 16) = (f32x4){bflo(e0[2]), bfhi(e0[2]), bflo(e0[3]), bfhi(e0[3])};
    }
    WAITV(4);
    v4u st = *(LAS v4u*)(lds + STAT + tg * 1024 + j32 * 32);
    LBAR();
#pragma nounroll
    for (unsigned spins = 0; spins < SPIN_CAP && !__all(st[1] == (unsigned)tag && st[3] == (unsigned)tag); ++spins)
      st = __builtin_amdgcn_raw_buffer_load_b128(rs_ws, WS_ST + (rt * 128 + tg * 32 + j32) * 32, 0, AUX_SC1);
    const f32x2 rstd = bc2(asf(st[0])), nmr = bc2(asf(st[2]));
#pragma nounroll
    for (int sg = 0; sg < 3; sg++) {
      LAS char* cp = lds + STAT + 4096 + (ch * 96 + sg * 32 + 16 * h) * 4;
      f32x4 c4v[4], b4v[4];
#pragma unroll
      for (int e4 = 0; e4 < 4; e4++) { c4v[e4] = *(LAS f32x4*)(cp + e4 * 16); b4v[e4] = *(LAS f32x4*)(cp + 1024 + e4 * 16); }
      if (sg < 2) { glds(rs_w2, (8 + sg) * PAGE + w * 1024, w2v, (8 + sg) * 131072); glds(rs_w2, (8 + sg) * PAGE + 8192 + w * 1024, w2v, (8 + sg) * 131072 + 65536); }
      SB();
      f32x2 v[8], t[8];
#pragma unroll
      for (int e = 0; e < 8; e++) {
        const f32x2 cc = {c4v[e >> 1][2 * (e & 1)], c4v[e >> 1][2 * (e & 1) + 1]}, bb = {b4v[e >> 1][2 * (e & 1)], b4v[e >> 1][2 * (e & 1) + 1]};
        v[e] = fma2((f32x2){acc[0][2 * e], acc[0][2 * e + 1]}, rstd, fma2(nmr, cc, bb));
      }
      SB();
#pragma unroll
      for (int e = 0; e < 8; e++) t[e] = v[e] * fma2(v[e] * v[e], bc2(-0.044715f * 2.302208198f), bc2(-2.302208198f));
      SB();
#pragma unroll
      for (int e = 0; e < 8; e++) t[e] = (f32x2){__builtin_amdgcn_exp2f(t[e][0]), __builtin_amdgcn_exp2f(t[e][1])} + 1.f;
      SB();
      v4u hv[2];
#pragma unroll
      for (int e = 0; e < 8; e++) hv[e >> 2][e & 3] = pk2(v[e] * (f32x2){__builtin_amdgcn_rcpf(t[e][0]), __builtin_amdgcn_rcpf(t[e][1])});
      const int lu = 2 * sg + ch;
      const unsigned so = (lu < 5 || grp < 3) ? (u0 + lu) * 2048 : WS_END;
      __builtin_amdgcn_raw_buffer_store_b128(hv[0], rs_ws, actw, so, AUX_SC1);
      __builtin_amdgcn_raw_buffer_store_b128(hv[1], rs_ws, actw + 1024, so, AUX_SC1);
      acc[0] = acc[1]; acc[1] = acc[2];
    }
    STAMP(2);
    publish((GAS u64*)(wsp + WS_HF) + ((rt * 4 + tg) * 16 + grp * 2 + ch), tag, lane);
    STAMP(3);
  } else {
    kloop(std::false_type{});
    STAMP(1);
    v4u ph[4];
    dual_softmax(mfma32(wp[1], pack8(acc[0], 8), mfma32(wp[0], pack8(acc[0], 0), zero16)), ph);
    LBAR();
    {
      LAS char* row = lds + IMG + ((2 * ch + h) * 128 + 32 * tg + j32) * 96;
#pragma unroll
      for (int e = 0; e < 4; e++) *(LAS v4u*)(row + 16 * e) = ph[e];
      *(LAS v4u*)(row + 64) = pack8(acc[1], 0);
      *(LAS v4u*)(row + 80) = pack8(acc[1], 8);
    }
    LBAR();
    STAMP(22);
    f32x4 kv = zero, zz = zero, kv2 = zero, zz2 = zero;
    LAS char* ib = lds + IMG + ((w >> 1) * 128 + 4 * g + q4) * 96 + 8 * p4;
#pragma unroll
    for (int ks = 0; ks < 4; ks++) {
      const v4u pa = trr2(ib + ks * 32 * 96 + 32 * (w & 1), 16 * 96), vb = trr2(ib + ks * 32 * 96 + 64, 16 * 96);
      if (ks & 1) { kv2 = mfma16(pa, vb, kv2); zz2 = mfma16(pa, ones, zz2); } else { kv = mfma16(pa, vb, kv); zz = mfma16(pa, ones, zz); }
    }
    kv += kv2; zz += zz2;
    __builtin_amdgcn_raw_buffer_store_b128(__builtin_bit_cast(v4u, kv), rs_ws, WS_KVP + ((grp * 16 + rt) * 8 + w) * 1024 + lane * 16, 0, AUX_SC1);
    if (j == 0) __builtin_amdgcn_raw_buffer_store_b128(__builtin_bit_cast(v4u, zz), rs_ws, WS_ZP + ((grp * 8 + w) * 16 + rt) * 64 + g * 16, 0, AUX_SC1);
    STAMP(2);
    publish((GAS u64*)(wsp + WS_KVF) + ((grp * 8 + w) * 16 + rt), tag, lane);
    STAMP(3);
  }

  const int th = w >> 2, q = w & 3;
  const int ab0 = q * 4096 + (16 * h + q4) * 128 + ((32 * blk + 8 * p4) ^ ((q4 & 2) << 5));
  LBAR();
  v4u wt[4][2][2];
#pragma unroll
  for (int r = 0; r < 4; r++)
#pragma unroll
    for (int s = 0; s < 2; s++)
#pragma unroll
      for (int c = 0; c < 2; c++) wt[r][s][c] = trr2(lds + r * PAGE + (ab0 ^ (64 * c)) + s * 1024, 512);
  LBAR();
  const int tgi = panel * 2 + th;
  const GAS u64* hline = (const GAS u64*)(wsp + WS_HF) + tgi * 16;
  bool hok = false;
  round_ld(7, 7); round_ld(10, 0);
  if (isM) {
    round_ld(11, 1); round_ld(12, 2); round_ld(13, 3);
    STAMP(4);
  } else {
    v4u ph[4];
    round_ld(8, 8);
    SB();
    dual_softmax(mfma32(wp[1], pack8(acc[2], 8), mfma32(wp[0], pack8(acc[2], 0), zero16)), ph);
    asm volatile("" : "+v"(ph[0]), "+v"(ph[1]), "+v"(ph[2]), "+v"(ph[3]));
    SB();
    round_ld(9, 9); round_ld(11, 1); round_ld(12, 2);
    STAMP(4);
    wait16((const GAS u64*)(wsp + WS_KVF) + (grp * 8 + w) * 16, tag, lane);
    STAMP(5);
    v4u pt[16];
#pragma unroll
    for (int r = 0; r < 16; r++) pt[r] = __builtin_amdgcn_raw_buffer_load_b128(rs_ws, WS_KVP + (grp * 16 * 8 + w) * 1024 + lane * 16, r * 8192, AUX_SC1);
    const v4u zr = __builtin_amdgcn_raw_buffer_load_b128(rs_ws, WS_ZP + (grp * 8 + w) * 1024 + lane * 16, 0, AUX_SC1);
    SB();
    f32x4 kv = __builtin_bit_cast(f32x4, pt[0]);
#pragma unroll
    for (int r = 1; r < 16; r++) kv += __builtin_bit_cast(f32x4, pt[r]);
    *(LAS v2u*)(lds + KVR + ((w >> 1) * 17 + j) * 64 + (16 * (w & 1) + 4 * g) * 2) = (v2u){pk(kv[0], kv[1]), pk(kv[2], kv[3])};
    f32x4 z = __builtin_bit_cast(f32x4, zr);
#pragma unroll
    for (int e = 0; e < 4; e++) {
      float v = z[e];
      v += asf(__builtin_amdgcn_update_dpp(0u, asu(v), 0x124, 0xf, 0xf, false));
      v += asf(__builtin_amdgcn_update_dpp(0u, asu(v), 0x128, 0xf, 0xf, false));
      z[e] = sum32(sum16(v));
    }
    if (lane < 4) *(LAS v2u*)(lds + KVR + ((w >> 1) * 17 + 16) * 64 + (16 * (w & 1) + 4 * lane) * 2) = (v2u){pk(z[0], z[1]), pk(z[2], z[3])};
    if (tid == 0) *(LAS v4u*)(lds + KVR_ZERO) = z4;
    LBAR();
    STAMP(6);
    const int hb = KVR + (2 * ch + h) * 17 * 64;
    const int ko = act ? hb + (4 * (j32 >> 3) + (j32 & 3)) * 64 : KVR_ZERO, zo = act ? hb + 16 * 64 : KVR_ZERO, st16 = act ? 16 : 0;
    f32x16 num = zero16, den = zero16;
#pragma unroll
    for (int e = 0; e < 4; e++) {
      num = mfma32(*(LAS v4u*)(lds + ko + e * st16), ph[e], num);
      den = mfma32(*(LAS v4u*)(lds + zo + e * st16), ph[e], den);
    }
    num *= __builtin_amdgcn_rcpf(den[0] + 1e-6f);
    __builtin_amdgcn_raw_buffer_store_b128(pack8(num, 0), rs_ws, actw, (44 + 2 * grp + ch) * 2048, AUX_SC1);
    __builtin_amdgcn_raw_buffer_store_b128(pack8(num, 8), rs_ws, actw + 1024, (44 + 2 * grp + ch) * 2048, AUX_SC1);
    hok = __all(__hip_atomic_load(hline + j, RLX_AGENT) == tag);
    publish((GAS u64*)(wsp + WS_OF) + ((rt * 4 + tg) * 16 + grp * 2 + ch), tag, lane);
    STAMP(7);
  }

  if (!hok) wait16(hline, tag, lane);
  __builtin_amdgcn_fence(__ATOMIC_ACQUIRE, "wavefront");
  STAMP(8);
  const unsigned av = WS_ACT + (tgi * 60 + q) * 2048 + lane * 16;
  v4u bf[11][2];
#pragma unroll
  for (int r = 0; r < 11; r++)
#pragma unroll
    for (int s = 0; s < 2; s++)
      bf[r][s] = __builtin_amdgcn_raw_buffer_load_b128(rs_ws, av + s * 1024, (r == 10 && q == 3) ? WS_END : r * 8192, AUX_SC1);
  SB();
  if (!isM) { LBAR(); round_ld(13, 3); }
  SB();
  f32x16 ob[2] = {zero16, zero16}, oa[2] = {zero16, zero16};
  v4u of[4][2], b2v; float gl, gt;
#pragma unroll
  for (int r = 0; r < 11; r++) {
    if (r == 7) {
      LBAR();
#pragma unroll
      for (int i = 0; i < 4; i++) glds(rs_wo, 4 * PAGE + w * 4096 + i * 1024, w2v, (384 + 32 * q + 8 * i - 8 * w) * 1024);
    }
    if (r == 9) {
      STAMP(9);
      if (b == 0 && w == 7) {
#pragma nounroll
        for (unsigned spins = 0; spins < SPIN_CAP; ++spins) {
          bool ok = true;
#pragma unroll
          for (int i = 0; i < 16; i++) ok &= __hip_atomic_load((const GAS u64*)(wsp + WS_HF) + i * 64 + lane, RLX_AGENT) == tag;
          if (__all(ok)) break;
          __builtin_amdgcn_s_sleep(1);
        }
      }
      wait16((const GAS u64*)(wsp + WS_OF) + tgi * 16, tag, lane);
      STAMP(10);
      if (b == 0 && w == 7 && lane == 0) __hip_atomic_store((GAS u64*)(wsp + WS_E), tag, RLX_AGENT);
#pragma unroll
      for (int k = 0; k < 4; k++)
#pragma unroll
        for (int s = 0; s < 2; s++) of[k][s] = __builtin_amdgcn_raw_buffer_load_b128(rs_ws, av + s * 1024, (11 + k) * 8192, AUX_SC1);
      gl = asf(__builtin_amdgcn_raw_buffer_load_b32(rs_ws, WS_ST + (tgi * 32 + j32) * 32 + 16, 0, AUX_SC1));
      SB();
    }
#pragma unroll
    for (int s = 0; s < 2; s++)
#pragma unroll
      for (int c = 0; c < 2; c++) {
        const v4u wf = r < 4 ? wt[r < 4 ? r : 0][s][c] : trr2(lds + (r < 10 ? r : 0) * PAGE + (ab0 ^ (64 * c)) + s * 1024, 512);
        ob[c] = mfma32(wf, bf[r][s], ob[c]);
      }
  }
  LBAR();
  gt = asf(__builtin_amdgcn_raw_buffer_load_b32(rs_ws, WS_ST + (panel * 64 + (tid >> 3)) * 32 + 16, 0, AUX_SC1));
  b2v = *(const GAS v4u*)((const GAS char*)p.b2 + (ct * 64 + (tid & 7) * 8) * 2);
  SB();
#pragma unroll
  for (int r = 0; r < 4; r++)
#pragma unroll
    for (int s = 0; s < 2; s++)
#pragma unroll
      for (int c = 0; c < 2; c++) {
        const v4u wf = trr2(lds + (r < 3 ? (r + 1) * PAGE : 4 * PAGE + (w - q) * 4096) + (ab0 ^ (64 * c)) + s * 1024, 512);
        oa[c] = mfma32(wf, of[r][s], oa[c]);
      }
  STAMP(11);
#pragma unroll
  for (int c = 0; c < 2; c++) {
    const f32x16 v = ob[c] + gl * (oa[c] - ob[c]);
#pragma unroll
    for (int rg = 0; rg < 4; rg++)
      *(LAS f32x4*)(lds + RED + (q * 64 + 32 * th + j32) * 256 + (((8 * c + 2 * rg + h) ^ (j32 & 7)) * 16)) = (f32x4){v[4 * rg], v[4 * rg + 1], v[4 * rg + 2], v[4 * rg + 3]};
  }
  LBAR();
  {
    const int tok = tid >> 3, c8 = tid & 7;
    const float og = 1.f - gt;
    f32x4 s0 = (f32x4){bflo(b2v[0]), bfhi(b2v[0]), bflo(b2v[1]), bfhi(b2v[1])} * og, s1 = (f32x4){bflo(b2v[2]), bfhi(b2v[2]), bflo(b2v[3]), bfhi(b2v[3])} * og;
#pragma unroll
    for (int qq = 0; qq < 4; qq++) {
      s0 += *(LAS f32x4*)(lds + RED + (qq * 64 + tok) * 256 + (((2 * c8) ^ (tok & 7)) * 16));
      s1 += *(LAS f32x4*)(lds + RED + (qq * 64 + tok) * 256 + (((2 * c8 + 1) ^ (tok & 7)) * 16));
    }
    *(GAS v4u*)((GAS char*)p.out + ((panel * 64 + tok) * 512 + ct * 64 + c8 * 8) * 2) = (v4u){pk(s0[0], s0[1]), pk(s0[2], s0[3]), pk(s1[0], s1[1]), pk(s1[2], s1[3])};
  }
  STAMP(12);
}
}

extern "C" void kernel_launch(void* const* d_in, const int* in_sizes, int n_in, void* d_out, int out_size, void* d_ws, size_t ws_size,
                              hipStream_t stream) {
  gm::Params p;
  p.x = d_in[0]; p.wqkv = d_in[1]; p.wphi = d_in[2]; p.wo = d_in[3];
  p.w1 = d_in[5]; p.b1 = d_in[6]; p.w2 = d_in[7]; p.b2 = d_in[8]; p.gw = d_in[9];
  p.out = d_out; p.ws = d_ws;
  gm::fused<<<256, 512, 0, stream>>>(p);
}
```

```cpp
#include <hip/hip_runtime.h>
#include <hip/hip_bf16.h>

typedef unsigned long long u64;
typedef unsigned v4u __attribute__((ext_vector_type(4)));
typedef unsigned v2u __attribute__((ext_vector_type(2)));
typedef float f32x16 __attribute__((ext_vector_type(16)));
typedef float f32x4 __attribute__((ext_vector_type(4)));
typedef float f32x2 __attribute__((ext_vector_type(2)));
typedef __bf16 bf16x8 __attribute__((ext_vector_type(8)));
typedef __bf16 bf16x2 __attribute__((ext_vector_type(2)));
typedef short s16x4 __attribute__((ext_vector_type(4)));
#define GAS __attribute__((address_space(1)))
#define LAS __attribute__((address_space(3)))
#define DI __device__ __forceinline__
#define RLX_AGENT __ATOMIC_RELAXED, __HIP_MEMORY_SCOPE_AGENT

#define STAMPS 0
#define LSTAMPS 0
#define CODE_KB 20

namespace gm {
constexpr int S = 2048, D = 512, F = 1376;
constexpr unsigned WS_E = 0, WS_KVF = 4096, WS_HF = 12288, WS_OF = 20480, WS_KVP = 131072, WS_ST = 1310720,
                   WS_ACT = 2u << 20, ACT_BYTES = 16u * 4 * 60 * 2048, WS_END = WS_ACT + ACT_BYTES, WS_STAMP = 16u << 20;
constexpr int SLOT = 40960, WOFF = 16384, PAGE = 16384;
constexpr int IMG = 7 * PAGE;
constexpr int KVR = 4 * PAGE;
constexpr int KVR_ZERO = KVR + 4 * 17 * 64;
constexpr int STAT = 7 * PAGE;
constexpr int RED = 6 * PAGE;
constexpr int AUX_SC1 = 16;
constexpr unsigned SPIN_CAP = 1u << 16;

struct Params {
  const void *x, *wqkv, *wphi, *wo, *w1, *b1, *w2, *b2, *gw;
  void *out, *ws;
};

DI float asf(unsigned u) { return __builtin_bit_cast(float, u); }
DI unsigned asu(float f) { return __builtin_bit_cast(unsigned, f); }
DI bf16x2 asb2(unsigned u) { return __builtin_bit_cast(bf16x2, u); }
DI f32x4 mfma16(v4u a, v4u b, f32x4 c) {
  return __builtin_amdgcn_mfma_f32_16x16x32_bf16(__builtin_bit_cast(bf16x8, a), __builtin_bit_cast(bf16x8, b), c, 0, 0, 0);
}
DI f32x16 mfma32(v4u a, v4u b, f32x16 c) {
  return __builtin_amdgcn_mfma_f32_32x32x16_bf16(__builtin_bit_cast(bf16x8, a), __builtin_bit_cast(bf16x8, b), c, 0, 0, 0);
}
DI unsigned pk(float a, float b) { return __builtin_bit_cast(unsigned, __builtin_convertvector((f32x2){a, b}, bf16x2)); }
DI unsigned pk2(f32x2 v) { return __builtin_bit_cast(unsigned, __builtin_convertvector(v, bf16x2)); }
DI float bflo(unsigned u) { return __builtin_bit_cast(float, u << 16); }
DI float bfhi(unsigned u) { return __builtin_bit_cast(float, u & 0xffff0000u); }
DI v2u trr(LAS char* p) { return __builtin_bit_cast(v2u, __builtin_amdgcn_ds_read_tr16_b64_v4i16((LAS s16x4*)p)); }
DI v4u trr2(LAS char* p, int off2) { v2u a = trr(p), b = trr(p + off2); return (v4u){a[0], a[1], b[0], b[1]}; }
DI f32x2 pair16(float v) { auto r = __builtin_amdgcn_permlane16_swap(asu(v), asu(v), false, false); return (f32x2){asf(r[0]), asf(r[1])}; }
DI f32x2 pair32(float v) { auto r = __builtin_amdgcn_permlane32_swap(asu(v), asu(v), false, false); return (f32x2){asf(r[0]), asf(r[1])}; }
DI float sum16(float v) { const f32x2 r = pair16(v); return r[0] + r[1]; }
DI float sum32(float v) { const f32x2 r = pair32(v); return r[0] + r[1]; }
DI f32x2 fma2(f32x2 a, f32x2 b, f32x2 c) { return __builtin_elementwise_fma(a, b, c); }
DI f32x2 bc2(float v) { return (f32x2){v, v}; }
#define WAITV(n) asm volatile("s_waitcnt vmcnt(%0)" ::"n"(n) : "memory")
#define WAITL() asm volatile("s_waitcnt lgkmcnt(0)" ::: "memory")
#define LBAR() do { WAITL(); __builtin_amdgcn_s_barrier(); asm volatile("" ::: "memory"); } while (0)
#define SB() __builtin_amdgcn_sched_barrier(0)
#define PIN8(a) asm volatile("" : "+v"(a[0]), "+v"(a[1]), "+v"(a[2]), "+v"(a[3]), "+v"(a[4]), "+v"(a[5]), "+v"(a[6]), "+v"(a[7]))

DI void glds(__amdgpu_buffer_rsrc_t rs, unsigned dst, unsigned voff, unsigned soff) {
  asm volatile("s_mov_b32 m0, %0\n\ts_nop 0\n\tbuffer_load_dwordx4 %1, %2, %3 offen lds" ::"s"(__builtin_amdgcn_readfirstlane(dst)), "v"(voff), "s"(rs), "s"(__builtin_amdgcn_readfirstlane(soff)) : "memory");
}
DI v4u pack8(const f32x16& a, int o) { return (v4u){pk(a[o], a[o + 1]), pk(a[o + 2], a[o + 3]), pk(a[o + 4], a[o + 5]), pk(a[o + 6], a[o + 7])}; }
template <class J> DI void dual_softmax(const f32x16& p, v4u (&o)[4], J&& job) {
  float mx = fmaxf(fmaxf(p[0], p[1]), p[2]), mn = fminf(fminf(p[0], p[1]), p[2]);
#pragma unroll
  for (int e = 3; e < 15; e += 2) { mx = fmaxf(fmaxf(mx, p[e]), p[e + 1]); mn = fminf(fminf(mn, p[e]), p[e + 1]); }
  mx = fmaxf(fmaxf(mx, p[15]), p[14]); mn = fminf(fminf(mn, p[15]), p[14]);
  constexpr float L2E = 1.4426950408889634f;
  float mxs = -mx * L2E, mns = mn * L2E;
  asm volatile("" : "+v"(mxs), "+v"(mns)); job(0); SB();
  f32x2 ep[8], em[8];
#pragma unroll
  for (int e = 0; e < 8; e++) {
    const f32x2 a = fma2((f32x2){p[2 * e], p[2 * e + 1]}, bc2(L2E), bc2(mxs));
    ep[e] = (f32x2){__builtin_amdgcn_exp2f(a[0]), __builtin_amdgcn_exp2f(a[1])};
  }
  PIN8(ep); job(1); SB();
#pragma unroll
  for (int e = 0; e < 8; e++) {
    const f32x2 c = fma2((f32x2){p[2 * e], p[2 * e + 1]}, bc2(-L2E), bc2(mns));
    em[e] = (f32x2){__builtin_amdgcn_exp2f(c[0]), __builtin_amdgcn_exp2f(c[1])};
  }
  PIN8(em); job(2); SB();
  const f32x2 sp = ((ep[0] + ep[1]) + (ep[2] + ep[3])) + ((ep[4] + ep[5]) + (ep[6] + ep[7]));
  const f32x2 sm = ((em[0] + em[1]) + (em[2] + em[3])) + ((em[4] + em[5]) + (em[6] + em[7]));
  float rp = __builtin_amdgcn_rcpf(sp[0] + sp[1]), rm = __builtin_amdgcn_rcpf(sm[0] + sm[1]);
  asm volatile("" : "+v"(rp), "+v"(rm)); job(3); SB();
#pragma unroll
  for (int e = 0; e < 8; e++) { o[e >> 2][e & 3] = pk2(ep[e] * rp); o[2 + (e >> 2)][e & 3] = pk2(em[e] * rm); }
}
DI void wait16(const GAS u64* line, u64 tag, int lane) {
#pragma nounroll
  for (unsigned spins = 0; spins < SPIN_CAP; ++spins) {
    const u64 v = __hip_atomic_load(line + (lane & 15), RLX_AGENT);
    if (__all(v == tag)) break;
    __builtin_amdgcn_s_sleep(1);
  }
  __builtin_amdgcn_fence(__ATOMIC_ACQUIRE, "wavefront");
}
DI void publish(GAS u64* flag, u64 tag, int lane) {
  WAITV(0);
  if (lane == 0) __hip_atomic_store(flag, tag, RLX_AGENT);
}

typedef const __attribute__((address_space(4))) Params* KP;
DI KP args_again() { KP k = (KP)__builtin_amdgcn_kernarg_segment_ptr(); asm volatile("" : "+s"(k)); return k; }

#define STAMP(i) do {} while (0)

__global__ void __launch_bounds__(512, 2) fused(Params p) {
  __shared__ __attribute__((aligned(16))) char smem[163840];
  LAS char* lds = (LAS char*)smem;
  const int tid = threadIdx.x, lane = tid & 63, w = __builtin_amdgcn_readfirstlane(tid >> 6);
  const int h = lane >> 5, j32 = lane & 31, blk = (lane >> 4) & 1, g = lane >> 4, j = lane & 15, q4 = (lane & 15) >> 2, p4 = lane & 3;
  const int tg = w >> 1, ch = w & 1;
  const int b = blockIdx.x, xcd = b & 7, idx = b >> 3;
  const int rt = (xcd >> 2) * 8 + (idx >> 2), c4 = idx & 3;
  const bool isM = c4 >= 2;
  const int grp = (xcd & 3) * 2 + (c4 & 1);
  const int u0 = grp < 3 ? 6 * grp : 3 + 5 * grp;
#define ROTX 2
  const int rot = ((idx >> 2) + ROTX * c4) & 7;
  const int panel = xcd * 4 + (idx >> 3), ct = idx & 7;
  STAMP(0);
  GAS char* wsp = (GAS char*)p.ws;
  if (w == 0 && idx < CODE_KB) {
    u64 pc;
    asm volatile("s_getpc_b64 %0" : "=s"(pc));
    asm volatile("s_mov_b32 m0, 0\n\ts_nop 0\n\tglobal_load_lds_dwordx4 %0, off" ::"v"((const GAS char*)(pc & ~255ull) + idx * 1024 + lane * 16) : "memory");
  }
  const __amdgpu_buffer_rsrc_t rs_ws = __builtin_amdgcn_make_buffer_rsrc(p.ws, 0, WS_END, 0x00020000);
  const __amdgpu_buffer_rsrc_t rs_w2 = __builtin_amdgcn_make_buffer_rsrc((void*)p.w2, 0, F * D * 2, 0x00020000);
  __amdgpu_buffer_rsrc_t rs_wo;
  const unsigned w2v = (8 * w + (lane >> 3)) * 1024 + ct * 128 + (((lane & 7) ^ ((lane >> 2) & 4)) * 16);
  const int P8 = 4 * blk + p4, cs1 = tg < 2 ? tg + 4 : tg;
  const bool act = ((lane >> 2) & 1) == h;
  f32x16 zero16;
#pragma unroll
  for (int e = 0; e < 16; e++) zero16[e] = 0.f;
  const f32x4 zero = {0.f, 0.f, 0.f, 0.f};
  const v4u ones = {0x3F803F80u, 0x3F803F80u, 0x3F803F80u, 0x3F803F80u}, z4 = {0u, 0u, 0u, 0u};
  f32x16 acc[3];
  f32x4 cs[2];
  v4u e0, wp[2];
  u64 tag;

  auto phase1 = [&](auto ism) {
    constexpr bool M = decltype(ism)::value;
    const __amdgpu_buffer_rsrc_t rs_x = __builtin_amdgcn_make_buffer_rsrc((void*)p.x, 0, S * D * 2, 0x00020000);
    const __amdgpu_buffer_rsrc_t rs_w = __builtin_amdgcn_make_buffer_rsrc((void*)(M ? p.w1 : p.wqkv), 0, M ? D * F * 2 : D * 1536 * 2, 0x00020000);
    v4u e1, xr; v2u ev;
    const int stok = rt * 128 + 32 * tg + 2 * (grp + (M ? 8 : 0)) + ch;
    {
      const int t = j < 6 ? j : 5, un = (t >= 4 && ch && grp >= 3) ? 0 : (t & 6) + ch;
      const unsigned off = M ? ((u0 + un) * 32 + 16 * h + 8 * (t & 1)) * 2 : (4 * grp + 2 * ch) * 512 + lane * 16;
      asm volatile("global_load_dwordx4 %0, %1, %2" : "=v"(xr) : "v"(stok * 1024 + lane * 16), "s"(p.x) : "memory");
      asm volatile("global_load_dwordx4 %0, %1, %2" : "=v"(e1) : "v"(lane * 16), "s"(p.gw) : "memory");
      asm volatile("global_load_dwordx2 %0, %1, %2 sc1" : "=v"(ev) : "v"(0), "s"(p.ws) : "memory");
      asm volatile("global_load_dwordx4 %0, %1, %2" : "=v"(e0) : "v"(off), "s"(M ? p.b1 : p.wphi) : "memory");
    }
    unsigned va[5];
#pragma unroll
    for (int i = 0; i < 2; i++) { const int row = 8 * (2 * w + i) + (lane >> 3); va[i] = (rt * 128 + row) * 1024 + (((lane & 7) ^ ((row >> 1) & 7)) * 16); }
    glds(rs_x, w * 2048, va[0], rot * 128);
    glds(rs_x, w * 2048 + 1024, va[1], rot * 128);
    constexpr unsigned wstr = M ? 2752 : 3072, wstep = 64 * wstr;
#pragma unroll
    for (int i = 0; i < 3; i++) {
      const unsigned n = (3 * w + i) * 64 + lane, k = n / 24, cp = n - k * 24;
      const unsigned c = cp ^ ((k & 2) << 1), seg = c >> 3;
      va[2 + i] = M ? k * wstr + u0 * 64 + c * 16 : k * wstr + (seg == 2 ? 0 : seg + 1) * 1024 + grp * 128 + (c & 7) * 16;
      glds(rs_w, WOFF + w * 3072 + i * 1024, va[2 + i], rot * wstep);
    }
    {
      const unsigned n = b * 8 + w;
      glds(__builtin_amdgcn_make_buffer_rsrc((void*)(n < 1376 ? p.w2 : p.wo), 0, n < 1376 ? F * D * 2 : D * D * 2, 0x00020000), SLOT + w * 2048, lane * 16, (n < 1376 ? n : n - 1376) * 1024);
      const unsigned fo = g == 0 ? WS_KVF + (grp * 8 + w) * 128 : WS_HF + (g - 1) * (WS_OF - WS_HF) + (panel * 2 + (w >> 2)) * 128;
      const u64 a0 = (u64)wsp + fo, a1 = (u64)p.b2 + ct * 128;
      const GAS char* a = (const GAS char*)(g == 3 ? a1 : a0);
      asm volatile("s_mov_b32 m0, %0\n\ts_nop 0\n\tglobal_load_lds_dword %1, off" ::"s"(SLOT + w * 2048 + 1024), "v"(a + j * 8) : "memory");
    }
    int xb0 = (32 * tg + j32) * 128 + ((h ^ ((j32 >> 1) & 7)) * 16);
    int wb0 = WOFF + (8 * h + q4) * 384 + ((ch * 64 + 32 * (P8 & 1) + 8 * (P8 >> 1)) ^ ((q4 & 2) << 5));
    const int crow = WOFF + (8 * g + q4) * 384;
    int cb0 = crow + (tg >> 1) * 128 + ((ch * 64 + 32 * (tg & 1) + 8 * p4) ^ ((q4 & 2) << 5));
    int cb1 = crow + (cs1 >> 1) * 128 + ((ch * 64 + 32 * (cs1 & 1) + 8 * p4) ^ ((q4 & 2) << 5));
    asm volatile("" : "+v"(xb0), "+v"(wb0));
    if (M) asm volatile("" : "+v"(cb0), "+v"(cb1));
    STAMP(21);
    asm volatile("s_waitcnt vmcnt(7)" : "+v"(e0), "+v"(e1), "+v"(xr), "+v"(ev) :: "memory");
    { u64 z = (((u64)(unsigned)__builtin_amdgcn_readfirstlane(ev[1]) << 32) | (unsigned)__builtin_amdgcn_readfirstlane(ev[0])) + 0x9E3779B97F4A7C15ull;
      z = (z ^ (z >> 30)) * 0xBF58476D1CE4E5B9ull; z = (z ^ (z >> 27)) * 0x94D049BB133111EBull; tag = z ^ (z >> 31); }
    if (!M) {
      LAS char* mine = lds + 3 * SLOT + w * 1024;
      *(LAS v4u*)(mine + lane * 16) = e0;
#pragma unroll
      for (int s = 0; s < 2; s++) wp[s] = trr2(mine + (h * 16 + 8 * s + q4) * 32 + 8 * (P8 >> 1), 128);
    }
    {
      float sv[3] = {0.f, 0.f, 0.f};
#pragma unroll
      for (int e = 0; e < 4; e++) {
        sv[0] = __builtin_amdgcn_fdot2_f32_bf16(asb2(xr[e]), asb2(0x3F803F80u), sv[0], false);
        sv[1] = __builtin_amdgcn_fdot2_f32_bf16(asb2(xr[e]), asb2(xr[e]), sv[1], false);
        sv[2] = __builtin_amdgcn_fdot2_f32_bf16(asb2(xr[e]), asb2(e1[e]), sv[2], false);
      }
#pragma unroll
      for (int e = 0; e < 3; e++) {
        float v = sv[e];
        v += asf(__builtin_amdgcn_update_dpp(0u, asu(v), 0x121, 0xf, 0xf, false));
        v += asf(__builtin_amdgcn_update_dpp(0u, asu(v), 0x122, 0xf, 0xf, false));
        v += asf(__builtin_amdgcn_update_dpp(0u, asu(v), 0x124, 0xf, 0xf, false));
        v += asf(__builtin_amdgcn_update_dpp(0u, asu(v), 0x128, 0xf, 0xf, false));
        sv[e] = sum32(sum16(v));
      }
      const float mean = sv[0] * (1.f / D);
      const float rstd = __builtin_amdgcn_rsqf(fmaxf(sv[1] * (1.f / D) - mean * mean, 0.f) + 1e-5f);
      const float gate = __builtin_amdgcn_rcpf(1.f + __builtin_amdgcn_exp2f(-1.4426950408889634f * sv[2]));
      const float val = lane == 0 ? rstd : lane == 1 ? -mean * rstd : gate;
      __builtin_amdgcn_raw_buffer_store_b64((v2u){asu(val), (unsigned)tag}, rs_ws, lane < 3 ? WS_ST + stok * 32 + lane * 8 : WS_END, 0, AUX_SC1);
    }
    if (!M) {
#pragma unroll
      for (int s = 0; s < 2; s++) {
#pragma unroll
        for (int e = 0; e < 4; e++) wp[s][e] = act ? wp[s][e] : 0u;
        asm volatile("" : "+v"(wp[s]));
      }
    }
    v4u xf[4], wf[4][3], cf[2][2];
    xf[2] = z4; xf[3] = z4; cf[1][0] = z4; cf[1][1] = z4; cs[0] = zero; cs[1] = zero;
#pragma unroll
    for (int n = 0; n < 5; n++) {
      if (n < 2) glds(rs_x, SLOT + w * 2048 + n * 1024, va[n], ((rot + 1) & 7) * 128);
      else glds(rs_w, SLOT + WOFF + w * 3072 + (n - 2) * 1024, va[n], ((rot + 1) & 7) * wstep);
      if (n < 3) { acc[n] = zero16; wf[2][n] = z4; wf[3][n] = z4; asm volatile("" : "+v"(acc[n]), "+v"(wf[2][n]), "+v"(wf[3][n])); }
      if (n == 3) { asm volatile("" : "+v"(xf[2]), "+v"(xf[3])); if (M) asm volatile("" : "+v"(cf[1][0]), "+v"(cf[1][1]), "+v"(cs[0]), "+v"(cs[1])); }
    }
    STAMP(13);
    __amdgpu_buffer_rsrc_t ra = rs_x, rb = rs_w;
    unsigned oA = w * 2048, oB = WOFF + w * 3072, dst = 1024, sst = 0, mulB = wstep / 128, addB = 0, sA = ((2 + rot) & 7) * 128, sinc = 128, smsk = 1023;
#pragma nounroll
    for (int i = 0; i < 8; i++) {
      const unsigned so = (i & 3) * SLOT;
      LAS char* wq = lds + so + wb0;
      if (__builtin_expect(i == 6, 0)) {
#pragma unroll
        for (int n = 0; n < 5; n++) va[n] = w2v;
        ra = rs_w2; rb = rs_w2; oA = w * 1024; oB = oA + 16384; dst = 8192; sst = 65536; mulB = 1; addB = 131072; sA = 0; sinc = 327680; smsk = ~0u;
      }
      WAITV(5);
      LBAR();
      auto rx = [&](int s) { xf[s] = *(LAS v4u*)(lds + ((so + xb0) ^ (32 * s))); };
      auto rw = [&](int s, int t) { wf[s][t] = trr2(wq + t * 128 + s * 6144, 1536); };
      auto mm = [&](int s, int t) { acc[t] = mfma32(wf[s][t], xf[s], acc[t]); SB(); };
      auto rc = [&](int ks) { if (M) { cf[ks][0] = trr2(lds + so + cb0 + ks * 12288, 1536); cf[ks][1] = trr2(lds + so + cb1 + ks * 12288, 1536); } };
      auto mc = [&](int ks) { if (M) { cs[0] = mfma16(cf[ks][0], ones, cs[0]); cs[1] = mfma16(cf[ks][1], ones, cs[1]); SB(); } };
      mm(2, 0); rx(0); rw(0, 0);
      const unsigned dsl = ((i + 2) & 3) * SLOT;
      const unsigned sB = sA * mulB + addB, dA = dsl + oA, dB = dsl + oB;
      if (M) { if (__builtin_expect(i == 7, 0)) asm volatile("s_mov_b32 m0, %0\n\ts_nop 0\n\tbuffer_load_dwordx4 %1, %2, %3 offen sc1 lds" ::"s"(STAT + tg * 1024), "v"(lane * 16), "s"(rs_ws), "s"(WS_ST + (rt * 128 + tg * 32) * 32) : "memory"); }
      SB();
      mm(2, 1); rw(0, 1); rw(0, 2); glds(ra, dA, va[0], sA); SB();
      mm(2, 2); rx(1); rw(1, 0); SB();
      mc(1);
      mm(3, 0); rw(1, 1); rw(1, 2); glds(ra, dA + dst, va[1], sA + sst); SB();
      mm(3, 1); rx(2); rw(2, 0); sA = (sA + sinc) & smsk; SB();
      mm(3, 2); rw(2, 1); rw(2, 2); glds(rb, dB, va[2], sB); SB();
      mm(0, 0); rx(3); rw(3, 0); SB();
      mm(0, 1); rw(3, 1); rw(3, 2); glds(rb, dB + dst, va[3], sB + sst); SB();
      mm(0, 2); rc(0); SB();
      mm(1, 0); rc(1); glds(rb, dB + 2 * dst, va[4], sB + 2 * sst); SB();
      mm(1, 1);
      mc(0);
      mm(1, 2);
    }
    rs_wo = __builtin_amdgcn_make_buffer_rsrc((void*)args_again()->wo, 0, D * D * 2, 0x00020000);
    if (M) { cs[0] = mfma16(cf[1][0], ones, cs[0]); cs[1] = mfma16(cf[1][1], ones, cs[1]); }
#pragma unroll
    for (int t = 0; t < 3; t++) { acc[t] = mfma32(wf[2][t], xf[2], acc[t]); acc[t] = mfma32(wf[3][t], xf[3], acc[t]); }
  };

  auto rpiece = [&](int r, int page, int n) {
    if (r < 11) glds(rs_w2, page * PAGE + n * 8192 + w * 1024, w2v, r * 131072 + n * 65536);
    else glds(rs_wo, page * PAGE + n * 8192 + w * 1024, w2v, (r - 11) * 131072 + n * 65536);
  };
  auto round_ld = [&](int r, int page) { rpiece(r, page, 0); rpiece(r, page, 1); };
  const unsigned actw = WS_ACT + (rt * 4 + tg) * 60 * 2048 + lane * 16;

  const int th = w >> 2, q = w & 3;
  const int ab0 = q * 4096 + (16 * h + q4) * 128 + ((32 * blk + 8 * p4) ^ ((q4 & 2) << 5));
  v4u wt[5][2][2];
  auto transit = [&]() {
#pragma unroll
    for (int r = 0; r < 5; r++)
#pragma unroll
      for (int s = 0; s < 2; s++)
#pragma unroll
        for (int c = 0; c < 2; c++) wt[r][s][c] = trr2(lds + r * PAGE + (ab0 ^ (64 * c)) + s * 1024, 512);
  };
  const int tgi = panel * 2 + th;
  const GAS u64* hline = (const GAS u64*)(wsp + WS_HF) + tgi * 16;
  bool hok = false;

  if (isM) {
    phase1(std::true_type{});
    STAMP(1);
    if (j == 0) {
      *(LAS f32x4*)(lds + STAT + 4096 + (ch * 96 + tg * 16 + 4 * g) * 4) = cs[0];
      *(LAS f32x4*)(lds + STAT + 4096 + (ch * 96 + cs1 * 16 + 4 * g) * 4) = cs[1];
    }
    if (j < 6 && blk == 0 && tg == 0) {
      LAS char* d = lds + STAT + 5120 + (ch * 96 + (j >> 1) * 32 + 16 * h + 8 * (j & 1)) * 4;
      *(LAS f32x4*)d = (f32x4){bflo(e0[0]), bfhi(e0[0]), bflo(e0[1]), bfhi(e0[1])};
      *(LAS f32x4*)(d + 16) = (f32x4){bflo(e0[2]), bfhi(e0[2]), bflo(e0[3]), bfhi(e0[3])};
    }
    WAITV(5);
    v4u st = *(LAS v4u*)(lds + STAT + tg * 1024 + j32 * 32);
    LBAR();
    f32x4 c4v[3][4], b4v[3][4];
    auto rcb = [&](int sg) {
      LAS char* cp = lds + STAT + 4096 + (ch * 96 + sg * 32 + 16 * h) * 4;
#pragma unroll
      for (int e4 = 0; e4 < 4; e4++) { c4v[sg][e4] = *(LAS f32x4*)(cp + e4 * 16); b4v[sg][e4] = *(LAS f32x4*)(cp + 1024 + e4 * 16); }
    };
    rcb(0);
    if (!__all(st[1] == (unsigned)tag && st[3] == (unsigned)tag)) {
#pragma nounroll
      for (unsigned spins = 0; spins < SPIN_CAP; ++spins) {
        const GAS u64* gp = (const GAS u64*)(wsp + WS_ST) + (rt * 128 + tg * 32 + j32) * 4;
        const u64 g0 = __hip_atomic_load(gp, RLX_AGENT), g1 = __hip_atomic_load(gp + 1, RLX_AGENT);
        st = (v4u){(unsigned)g0, (unsigned)(g0 >> 32), (unsigned)g1, (unsigned)(g1 >> 32)};
        if (__all(st[1] == (unsigned)tag && st[3] == (unsigned)tag)) break;
      }
    }
    const f32x2 rstd = bc2(asf(st[0])), nmr = bc2(asf(st[2]));
    auto unit = [&](int sg, auto&& j0, auto&& j1, auto&& j2) {
      f32x2 v[8], t[8];
#pragma unroll
      for (int e = 0; e < 8; e++) {
        const f32x2 cc = {c4v[sg][e >> 1][2 * (e & 1)], c4v[sg][e >> 1][2 * (e & 1) + 1]}, bb = {b4v[sg][e >> 1][2 * (e & 1)], b4v[sg][e >> 1][2 * (e & 1) + 1]};
        t[e] = fma2(nmr, cc, bb);
      }
#pragma unroll
      for (int e = 0; e < 8; e++) v[e] = fma2((f32x2){acc[sg][2 * e], acc[sg][2 * e + 1]}, rstd, t[e]);
      PIN8(v); j0(); SB();
#pragma unroll
      for (int e = 0; e < 8; e++) t[e] = v[e] * fma2(v[e] * v[e], bc2(-0.044715f * 2.302208198f), bc2(-2.302208198f));
      PIN8(t); j1(); SB();
#pragma unroll
      for (int e = 0; e < 8; e++) t[e] = (f32x2){__builtin_amdgcn_exp2f(t[e][0]), __builtin_amdgcn_exp2f(t[e][1])} + 1.f;
      PIN8(t); j2(); SB();
      v4u hv[2];
#pragma unroll
      for (int e = 0; e < 8; e++) hv[e >> 2][e & 3] = pk2(v[e] * (f32x2){__builtin_amdgcn_rcpf(t[e][0]), __builtin_amdgcn_rcpf(t[e][1])});
      const int lu = 2 * sg + ch;
      const unsigned so = (lu < 5 || grp < 3) ? (u0 + lu) * 2048 : WS_END;
      __builtin_amdgcn_raw_buffer_store_b128(hv[0], rs_ws, actw, so, AUX_SC1);
      __builtin_amdgcn_raw_buffer_store_b128(hv[1], rs_ws, actw + 1024, so, AUX_SC1);
      SB();
    };
    rcb(1);
    unit(0, [&]() { rpiece(5, 5, 0); }, [&]() { rpiece(5, 5, 1); }, [&]() { rpiece(6, 6, 0); });
    rcb(2);
    unit(1, [&]() { rpiece(6, 6, 1); }, [&]() { rpiece(8, 8, 0); }, [&]() { rpiece(8, 8, 1); });
    unit(2, [&]() { rpiece(9, 9, 0); }, [&]() { rpiece(9, 9, 1); }, [&]() {});
    STAMP(2);
    WAITV(14);
    LBAR();
    transit();
    publish((GAS u64*)(wsp + WS_HF) + ((rt * 4 + tg) * 16 + grp * 2 + ch), tag, lane);
    STAMP(3);
    LBAR();
    round_ld(7, 7); round_ld(10, 0); round_ld(11, 1); round_ld(12, 2);
    STAMP(4);
  } else {
    phase1(std::false_type{});
    STAMP(1);
    LBAR();
    v4u ph[4];
    LAS char* row = lds + IMG + ((2 * ch + h) * 128 + 32 * tg + j32) * 96;
    const f32x16 pkk = mfma32(wp[1], pack8(acc[0], 8), mfma32(wp[0], pack8(acc[0], 0), zero16));
    *(LAS v4u*)(row + 64) = pack8(acc[1], 0);
    *(LAS v4u*)(row + 80) = pack8(acc[1], 8);
    dual_softmax(pkk, ph, [&](int n) { rpiece(5 + (n >> 1), 5 + (n >> 1), n & 1); });
#pragma unroll
    for (int e = 0; e < 4; e++) *(LAS v4u*)(row + 16 * e) = ph[e];
    LBAR();
    STAMP(22);
    f32x4 kv = zero, zz = zero, kv2 = zero, zz2 = zero;
    LAS char* ib = lds + IMG + ((w >> 1) * 128 + 4 * g + q4) * 96 + 8 * p4;
#pragma unroll
    for (int ks = 0; ks < 4; ks++) {
      const v4u pa = trr2(ib + ks * 32 * 96 + 32 * (w & 1), 16 * 96), vb = trr2(ib + ks * 32 * 96 + 64, 16 * 96);
      if (ks & 1) { kv2 = mfma16(pa, vb, kv2); zz2 = mfma16(pa, ones, zz2); } else { kv = mfma16(pa, vb, kv); zz = mfma16(pa, ones, zz); }
    }
    kv += kv2; zz += zz2;
    const unsigned kvb = WS_KVP + (grp * 8 + w) * 16 * 640;
    {
      const f32x4 sel = (g & 1) ? zz : kv, oth = (g & 1) ? kv : zz;
      const unsigned a0 = pk(sel[0], sel[1]), a1 = pk(sel[2], sel[3]), b0 = pk(oth[0], oth[1]), b1 = pk(oth[2], oth[3]);
      const auto r0 = __builtin_amdgcn_permlane16_swap(b0, b0 ^ 0u, false, false), r1 = __builtin_amdgcn_permlane16_swap(b1, b1 ^ 0u, false, false);
      const unsigned p0 = (g & 1) ? r0[0] : r0[1], p1 = (g & 1) ? r1[0] : r1[1];
      const v4u piece = (g & 1) ? (v4u){p0, p1, a0, a1} : (v4u){a0, a1, p0, p1};
      const unsigned off = (g & 1) ? (j == 0 ? kvb + rt * 640 + 512 + (g >> 1) * 16 : WS_END) : kvb + rt * 640 + ((g >> 1) * 16 + j) * 16;
      __builtin_amdgcn_raw_buffer_store_b128(piece, rs_ws, off, 0, AUX_SC1);
    }
    const f32x16 pkq = mfma32(wp[1], pack8(acc[2], 8), mfma32(wp[0], pack8(acc[2], 0), zero16));
    STAMP(2);
    WAITV(5);
    LBAR();
    transit();
    publish((GAS u64*)(wsp + WS_KVF) + ((grp * 8 + w) * 16 + rt), tag, lane);
    STAMP(3);
    LBAR();
    dual_softmax(pkq, ph, [&](int n) { if (n < 3) round_ld(7 + n, 7 + n); if (n < 2) rpiece(10, 0, n); });
    asm volatile("" : "+v"(ph[0]), "+v"(ph[1]), "+v"(ph[2]), "+v"(ph[3]));
    v4u sela;
#pragma unroll
    for (int e = 0; e < 4; e++) sela[e] = j == 8 * (g >> 1) + 2 * e ? 0x3F80u : j == 8 * (g >> 1) + 2 * e + 1 ? 0x3F800000u : 0u;
    asm volatile("" : "+v"(sela));
    STAMP(4);
    wait16((const GAS u64*)(wsp + WS_KVF) + (grp * 8 + w) * 16, tag, lane);
    STAMP(5);
    v4u pt[8];
#pragma unroll
    for (int r = 0; r < 8; r++) pt[r] = __builtin_amdgcn_raw_buffer_load_b128(rs_ws, kvb + (g & 1) * 8 * 640 + ((g >> 1) * 16 + j) * 16, r * 640, AUX_SC1);
    const v4u zr = __builtin_amdgcn_raw_buffer_load_b128(rs_ws, kvb + ((g & 1) * 8 + (j & 7)) * 640 + 512 + (g >> 1) * 16, 0, AUX_SC1);
    SB();
    kv = zero; kv2 = zero;
#pragma unroll
    for (int r = 0; r < 8; r += 2) { kv = mfma16(sela, pt[r], kv); kv2 = mfma16(sela, pt[r + 1], kv2); }
    f32x4 z = mfma16(sela, zr, zero);
    kv += kv2;
    *(LAS v2u*)(lds + KVR + ((w >> 1) * 17 + j) * 64 + (16 * (w & 1) + 4 * g) * 2) = (v2u){pk(kv[0], kv[1]), pk(kv[2], kv[3])};
#pragma unroll
    for (int e = 0; e < 4; e++) {
      float v = z[e];
      v += asf(__builtin_amdgcn_update_dpp(0u, asu(v), 0x121, 0xf, 0xf, false));
      v += asf(__builtin_amdgcn_update_dpp(0u, asu(v), 0x122, 0xf, 0xf, false));
      v += asf(__builtin_amdgcn_update_dpp(0u, asu(v), 0x124, 0xf, 0xf, false));
      z[e] = v;
    }
    if (j == 0) *(LAS v2u*)(lds + KVR + ((w >> 1) * 17 + 16) * 64 + (16 * (w & 1) + 4 * g) * 2) = (v2u){pk(z[0], z[1]), pk(z[2], z[3])};
    if (tid == 0) *(LAS v4u*)(lds + KVR_ZERO) = z4;
    LBAR();
    STAMP(6);
    const int hb = KVR + (2 * ch + h) * 17 * 64;
    const int ko = act ? hb + (4 * (j32 >> 3) + (j32 & 3)) * 64 : KVR_ZERO, zo = act ? hb + 16 * 64 : KVR_ZERO, st16 = act ? 16 : 0;
    f32x16 num = zero16, den = zero16;
#pragma unroll
    for (int e = 0; e < 4; e++) {
      num = mfma32(*(LAS v4u*)(lds + ko + e * st16), ph[e], num);
      den = mfma32(*(LAS v4u*)(lds + zo + e * st16), ph[e], den);
    }
    num *= __builtin_amdgcn_rcpf(den[0] + 1e-6f);
    __builtin_amdgcn_raw_buffer_store_b128(pack8(num, 0), rs_ws, actw, (44 + 2 * grp + ch) * 2048, AUX_SC1);
    __builtin_amdgcn_raw_buffer_store_b128(pack8(num, 8), rs_ws, actw + 1024, (44 + 2 * grp + ch) * 2048, AUX_SC1);
    u64 hv;
    asm volatile("global_load_dwordx2 %0, %1, off sc1" : "=v"(hv) : "v"(hline + j) : "memory");
    round_ld(11, 1); round_ld(12, 2);
    asm volatile("s_waitcnt vmcnt(4)" : "+v"(hv) :: "memory");
    if (lane == 0) __hip_atomic_store((GAS u64*)(wsp + WS_OF) + ((rt * 4 + tg) * 16 + grp * 2 + ch), tag, RLX_AGENT);
    hok = __all(hv == tag);
    STAMP(7);
  }

  if (!hok) wait16(hline, tag, lane);
  __builtin_amdgcn_fence(__ATOMIC_ACQUIRE, "wavefront");
  STAMP(8);
  const unsigned av = WS_ACT + (tgi * 60 + q) * 2048 + lane * 16;
  v4u bf[11][2];
#pragma unroll
  for (int r = 0; r < 11; r++)
#pragma unroll
    for (int s = 0; s < 2; s++)
      bf[r][s] = __builtin_amdgcn_raw_buffer_load_b128(rs_ws, av + s * 1024, (r == 10 && q == 3) ? WS_END : r * 8192, AUX_SC1);
  SB();
  if (!isM) LBAR();
  SB();
  f32x16 ob[2] = {zero16, zero16}, oa[2] = {zero16, zero16};
  v4u of[4][2], b2v; float gl, gt; void* outp; const void* b2p;
#pragma unroll
  for (int r = 0; r < 11; r++) {
    if (r == 1 || r == 2) { SB(); round_ld(12 + r, 2 + r); SB(); }
    if (r == 7) { LBAR(); const KP k = args_again(); outp = k->out; b2p = k->b2; }
    if (r == 9) {
      STAMP(9);
      if (b == 0 && w == 7) {
#pragma nounroll
        for (unsigned spins = 0; spins < SPIN_CAP; ++spins) {
          bool ok = true;
#pragma unroll
          for (int i = 0; i < 16; i++) ok &= __hip_atomic_load((const GAS u64*)(wsp + WS_HF) + i * 64 + lane, RLX_AGENT) == tag;
          if (__all(ok)) break;
          __builtin_amdgcn_s_sleep(1);
        }
      }
      wait16((const GAS u64*)(wsp + WS_OF) + tgi * 16, tag, lane);
      STAMP(10);
      if (b == 0 && w == 7 && lane == 0) __hip_atomic_store((GAS u64*)(wsp + WS_E), tag, RLX_AGENT);
#pragma unroll
      for (int k = 0; k < 4; k++)
#pragma unroll
        for (int s = 0; s < 2; s++) of[k][s] = __builtin_amdgcn_raw_buffer_load_b128(rs_ws, av + s * 1024, (11 + k) * 8192, AUX_SC1);
      gl = asf(__builtin_amdgcn_raw_buffer_load_b32(rs_ws, WS_ST + (tgi * 32 + j32) * 32 + 16, 0, AUX_SC1));
      SB();
    }
    if (r == 10) {
      LBAR();
      gt = asf(__builtin_amdgcn_raw_buffer_load_b32(rs_ws, WS_ST + (panel * 64 + (tid >> 3)) * 32 + 16, 0, AUX_SC1));
      b2v = *(const GAS v4u*)((const GAS char*)b2p + (ct * 64 + (tid & 7) * 8) * 2);
      SB();
    }
#pragma unroll
    for (int s = 0; s < 2; s++)
#pragma unroll
      for (int c = 0; c < 2; c++) {
        const v4u wf = r < 5 ? wt[r < 5 ? r : 0][s][c] : trr2(lds + (r < 10 ? r : 0) * PAGE + (ab0 ^ (64 * c)) + s * 1024, 512);
        ob[c] = mfma32(wf, bf[r][s], ob[c]);
      }
  }
#pragma unroll
  for (int r = 0; r < 4; r++)
#pragma unroll
    for (int s = 0; s < 2; s++)
#pragma unroll
      for (int c = 0; c < 2; c++) {
        const v4u wf = trr2(lds + (r + 1) * PAGE + (ab0 ^ (64 * c)) + s * 1024, 512);
        oa[c] = mfma32(wf, of[r][s], oa[c]);
      }
  STAMP(11);
#pragma unroll
  for (int c = 0; c < 2; c++) {
    const f32x16 v = ob[c] + gl * (oa[c] - ob[c]);
#pragma unroll
    for (int rg = 0; rg < 4; rg++)
      *(LAS f32x4*)(lds + RED + (q * 64 + 32 * th + j32) * 256 + (((8 * c + 2 * rg + h) ^ (j32 & 7)) * 16)) = (f32x4){v[4 * rg], v[4 * rg + 1], v[4 * rg + 2], v[4 * rg + 3]};
  }
  LBAR();
  {
    const int tok = tid >> 3, c8 = tid & 7;
    const float og = 1.f - gt;
    f32x4 s0 = (f32x4){bflo(b2v[0]), bfhi(b2v[0]), bflo(b2v[1]), bfhi(b2v[1])} * og, s1 = (f32x4){bflo(b2v[2]), bfhi(b2v[2]), bflo(b2v[3]), bfhi(b2v[3])} * og;
#pragma unroll
    for (int qq = 0; qq < 4; qq++) {
      s0 += *(LAS f32x4*)(lds + RED + (qq * 64 + tok) * 256 + (((2 * c8) ^ (tok & 7)) * 16));
      s1 += *(LAS f32x4*)(lds + RED + (qq * 64 + tok) * 256 + (((2 * c8 + 1) ^ (tok & 7)) * 16));
    }
    *(GAS v4u*)((GAS char*)outp + ((panel * 64 + tok) * 512 + ct * 64 + c8 * 8) * 2) = (v4u){pk(s0[0], s0[1]), pk(s0[2], s0[3]), pk(s1[0], s1[1]), pk(s1[2], s1[3])};
  }
  STAMP(12);
}
}

extern "C" void kernel_launch(void* const* d_in, const int* in_sizes, int n_in, void* d_out, int out_size, void* d_ws, size_t ws_size,
                              hipStream_t stream) {
  gm::Params p;
  p.x = d_in[0]; p.wqkv = d_in[1]; p.wphi = d_in[2]; p.wo = d_in[3];
  p.w1 = d_in[5]; p.b1 = d_in[6]; p.w2 = d_in[7]; p.b2 = d_in[8]; p.gw = d_in[9];
  p.out = d_out; p.ws = d_ws;
  gm::fused<<<256, 512, 0, stream>>>(p);
}
```

```cpp
#define OUT_AUX 16
#include <hip/hip_runtime.h>
#include <hip/hip_bf16.h>

typedef unsigned long long u64;
typedef unsigned v4u __attribute__((ext_vector_type(4)));
typedef unsigned v2u __attribute__((ext_vector_type(2)));
typedef float f32x16 __attribute__((ext_vector_type(16)));
typedef float f32x4 __attribute__((ext_vector_type(4)));
typedef float f32x2 __attribute__((ext_vector_type(2)));
typedef __bf16 bf16x8 __attribute__((ext_vector_type(8)));
typedef __bf16 bf16x2 __attribute__((ext_vector_type(2)));
typedef short s16x4 __attribute__((ext_vector_type(4)));
#define GAS __attribute__((address_space(1)))
#define LAS __attribute__((address_space(3)))
#define DI __device__ __forceinline__
#define RLX_AGENT __ATOMIC_RELAXED, __HIP_MEMORY_SCOPE_AGENT

#define STAMPS 0
#define LSTAMPS 0
#define TOUCH 0
#define WIN 3
#define CODE_KB 22

namespace gm {
constexpr int S = 2048, D = 512, F = 1376;
constexpr unsigned WS_E = 0, WS_KVF = 4096, WS_HF = 12288, WS_OF = 20480, WS_KVP = 131072, WS_ST = 1310720,
                   WS_ACT = 2u << 20, ACT_BYTES = 16u * 4 * 60 * 2048, WS_END = WS_ACT + ACT_BYTES, WS_STAMP = 16u << 20;
constexpr int SLOT = 40960, WOFF = 16384, PAGE = 16384;
constexpr int IMG = 7 * PAGE;
constexpr int KVR = 4 * PAGE;
constexpr int KVR_ZERO = KVR + 4 * 17 * 64;
constexpr int STAT = 7 * PAGE;
constexpr int RED = 6 * PAGE;
constexpr int AUX_SC1 = 16;
constexpr unsigned SPIN_CAP = 1u << 16;

struct Params {
  const void *x, *wqkv, *wphi, *wo, *w1, *b1, *w2, *b2, *gw;
  void *out, *ws;
};

DI float asf(unsigned u) { return __builtin_bit_cast(float, u); }
DI unsigned asu(float f) { return __builtin_bit_cast(unsigned, f); }
DI bf16x2 asb2(unsigned u) { return __builtin_bit_cast(bf16x2, u); }
DI f32x4 mfma16(v4u a, v4u b, f32x4 c) {
  return __builtin_amdgcn_mfma_f32_16x16x32_bf16(__builtin_bit_cast(bf16x8, a), __builtin_bit_cast(bf16x8, b), c, 0, 0, 0);
}
DI f32x16 mfma32(v4u a, v4u b, f32x16 c) {
  return __builtin_amdgcn_mfma_f32_32x32x16_bf16(__builtin_bit_cast(bf16x8, a), __builtin_bit_cast(bf16x8, b), c, 0, 0, 0);
}
DI unsigned pk(float a, float b) { return __builtin_bit_cast(unsigned, __builtin_convertvector((f32x2){a, b}, bf16x2)); }
DI unsigned pk2(f32x2 v) { return __builtin_bit_cast(unsigned, __builtin_convertvector(v, bf16x2)); }
DI float bflo(unsigned u) { return __builtin_bit_cast(float, u << 16); }
DI float bfhi(unsigned u) { return __builtin_bit_cast(float, u & 0xffff0000u); }
DI v2u trr(LAS char* p) { return __builtin_bit_cast(v2u, __builtin_amdgcn_ds_read_tr16_b64_v4i16((LAS s16x4*)p)); }
DI v4u trr2(LAS char* p, int off2) { v2u a = trr(p), b = trr(p + off2); return (v4u){a[0], a[1], b[0], b[1]}; }
DI f32x2 pair16(float v) { auto r = __builtin_amdgcn_permlane16_swap(asu(v), asu(v), false, false); return (f32x2){asf(r[0]), asf(r[1])}; }
DI f32x2 pair32(float v) { auto r = __builtin_amdgcn_permlane32_swap(asu(v), asu(v), false, false); return (f32x2){asf(r[0]), asf(r[1])}; }
DI float sum16(float v) { const f32x2 r = pair16(v); return r[0] + r[1]; }
DI float sum32(float v) { const f32x2 r = pair32(v); return r[0] + r[1]; }
DI f32x2 fma2(f32x2 a, f32x2 b, f32x2 c) { return __builtin_elementwise_fma(a, b, c); }
DI f32x2 bc2(float v) { return (f32x2){v, v}; }
#define WAITV(n) asm volatile("s_waitcnt vmcnt(%0)" ::"n"(n) : "memory")
#define WAITL() asm volatile("s_waitcnt lgkmcnt(0)" ::: "memory")
#define LBAR() do { WAITL(); __builtin_amdgcn_s_barrier(); asm volatile("" ::: "memory"); } while (0)
#define SB() __builtin_amdgcn_sched_barrier(0)
#define PIN8(a) asm volatile("" : "+v"(a[0]), "+v"(a[1]), "+v"(a[2]), "+v"(a[3]), "+v"(a[4]), "+v"(a[5]), "+v"(a[6]), "+v"(a[7]))

DI void glds(__amdgpu_buffer_rsrc_t rs, unsigned dst, unsigned voff, unsigned soff) {
  asm volatile("s_mov_b32 m0, %0\n\ts_nop 0\n\tbuffer_load_dwordx4 %1, %2, %3 offen lds" ::"s"(__builtin_amdgcn_readfirstlane(dst)), "v"(voff), "s"(rs), "s"(__builtin_amdgcn_readfirstlane(soff)) : "memory");
}
DI v4u pack8(const f32x16& a, int o) { return (v4u){pk(a[o], a[o + 1]), pk(a[o + 2], a[o + 3]), pk(a[o + 4], a[o + 5]), pk(a[o + 6], a[o + 7])}; }
template <class J> DI void dual_softmax(const f32x16& p, v4u (&o)[4], J&& job) {
  float mx = fmaxf(fmaxf(p[0], p[1]), p[2]), mn = fminf(fminf(p[0], p[1]), p[2]);
#pragma unroll
  for (int e = 3; e < 15; e += 2) { mx = fmaxf(fmaxf(mx, p[e]), p[e + 1]); mn = fminf(fminf(mn, p[e]), p[e + 1]); }
  mx = fmaxf(fmaxf(mx, p[15]), p[14]); mn = fminf(fminf(mn, p[15]), p[14]);
  constexpr float L2E = 1.4426950408889634f;
  float ms[2] = {-mx * L2E, mn * L2E};
  asm volatile("" : "+v"(ms[0]), "+v"(ms[1])); job(0); SB();
#pragma unroll
  for (int sg = 0; sg < 2; sg++) {
    f32x2 ex[8];
#pragma unroll
    for (int e = 0; e < 8; e++) {
      const f32x2 a = fma2((f32x2){p[2 * e], p[2 * e + 1]}, bc2(sg ? -L2E : L2E), bc2(ms[sg]));
      ex[e] = (f32x2){__builtin_amdgcn_exp2f(a[0]), __builtin_amdgcn_exp2f(a[1])};
    }
    PIN8(ex); job(1 + 2 * sg); SB();
    const f32x2 sp = ((ex[0] + ex[1]) + (ex[2] + ex[3])) + ((ex[4] + ex[5]) + (ex[6] + ex[7]));
    const float rp = __builtin_amdgcn_rcpf(sp[0] + sp[1]);
#pragma unroll
    for (int e = 0; e < 8; e++) o[2 * sg + (e >> 2)][e & 3] = pk2(ex[e] * rp);
    if (sg == 0) { asm volatile("" : "+v"(o[0]), "+v"(o[1])); job(2); SB(); }
  }
}
DI void wait16(const GAS u64* line, u64 tag, int lane) {
#pragma nounroll
  for (unsigned spins = 0; spins < SPIN_CAP; ++spins) {
    const u64 v = __hip_atomic_load(line + (lane & 15), RLX_AGENT);
    if (__all(v == tag)) break;
    __builtin_amdgcn_s_sleep(1);
  }
  __builtin_amdgcn_fence(__ATOMIC_ACQUIRE, "wavefront");
}
DI void publish(GAS u64* flag, u64 tag, int lane) {
  WAITV(0);
  if (lane == 0) __hip_atomic_store(flag, tag, RLX_AGENT);
}

typedef const __attribute__((address_space(4))) Params* KP;
DI KP args_again() { KP k = (KP)__builtin_amdgcn_kernarg_segment_ptr(); asm volatile("" : "+s"(k)); return k; }

#define STAMP(i) do {} while (0)

__global__ void __launch_bounds__(512, 2) fused(Params p) {
  __shared__ __attribute__((aligned(16))) char smem[163840];
  LAS char* lds = (LAS char*)smem;
  const int tid = threadIdx.x, lane = tid & 63, w = __builtin_amdgcn_readfirstlane(tid >> 6);
  const int h = lane >> 5, j32 = lane & 31, blk = (lane >> 4) & 1, g = lane >> 4, j = lane & 15, q4 = (lane & 15) >> 2, p4 = lane & 3;
  const int tg = w >> 1, ch = w & 1;
  const int b = blockIdx.x, xcd = b & 7, idx = b >> 3;
  const int rt = (xcd >> 2) * 8 + (idx >> 2), c4 = idx & 3;
  const bool isM = c4 >= 2;
  const int grp = (xcd & 3) * 2 + (c4 & 1);
  const int u0 = grp < 3 ? 6 * grp : 3 + 5 * grp;
#define ROTX 2
  const int rot = ((idx >> 2) + ROTX * c4) & 7;
  const int panel = xcd * 4 + (idx >> 3), ct = idx & 7;
  STAMP(0);
  GAS char* wsp = (GAS char*)p.ws;
  if (w == 0 && idx < CODE_KB) {
    u64 pc;
    asm volatile("s_getpc_b64 %0" : "=s"(pc));
    asm volatile("s_mov_b32 m0, 0\n\ts_nop 0\n\tglobal_load_lds_dwordx4 %0, off" ::"v"((const GAS char*)(pc & ~255ull) + idx * 1024 + lane * 16) : "memory");
  }
  const __amdgpu_buffer_rsrc_t rs_ws = __builtin_amdgcn_make_buffer_rsrc(p.ws, 0, WS_END, 0x00020000);
  const __amdgpu_buffer_rsrc_t rs_w2 = __builtin_amdgcn_make_buffer_rsrc((void*)p.w2, 0, F * D * 2, 0x00020000);
  __amdgpu_buffer_rsrc_t rs_wo;
  const unsigned w2v = (8 * w + (lane >> 3)) * 1024 + ct * 128 + (((lane & 7) ^ ((lane >> 2) & 4)) * 16);
  const int P8 = 4 * blk + p4, cs1 = tg < 2 ? tg + 4 : tg;
  const bool act = ((lane >> 2) & 1) == h;
  f32x16 zero16;
#pragma unroll
  for (int e = 0; e < 16; e++) zero16[e] = 0.f;
  const f32x4 zero = {0.f, 0.f, 0.f, 0.f};
  const v4u ones = {0x3F803F80u, 0x3F803F80u, 0x3F803F80u, 0x3F803F80u}, z4 = {0u, 0u, 0u, 0u};
  f32x16 acc[3];
  f32x4 cs[2];
  v4u e0, wp[2];
  u64 tag;

  auto phase1 = [&](auto ism) {
    constexpr bool M = decltype(ism)::value;
    const __amdgpu_buffer_rsrc_t rs_x = __builtin_amdgcn_make_buffer_rsrc((void*)p.x, 0, S * D * 2, 0x00020000);
    const __amdgpu_buffer_rsrc_t rs_w = __builtin_amdgcn_make_buffer_rsrc((void*)(M ? p.w1 : p.wqkv), 0, M ? D * F * 2 : D * 1536 * 2, 0x00020000);
    v4u e1, xr; v2u ev;
    const int stok = rt * 128 + 32 * tg + 2 * (grp + (M ? 8 : 0)) + ch;
    {
      const int t = j < 6 ? j : 5, un = (t >= 4 && ch && grp >= 3) ? 0 : (t & 6) + ch;
      const unsigned off = M ? ((u0 + un) * 32 + 16 * h + 8 * (t & 1)) * 2 : (4 * grp + 2 * ch) * 512 + lane * 16;
      asm volatile("global_load_dwordx4 %0, %1, %2" : "=v"(xr) : "v"(stok * 1024 + lane * 16), "s"(p.x) : "memory");
      asm volatile("global_load_dwordx4 %0, %1, %2" : "=v"(e1) : "v"(lane * 16), "s"(p.gw) : "memory");
      asm volatile("global_load_dwordx2 %0, %1, %2 sc1" : "=v"(ev) : "v"(0), "s"(p.ws) : "memory");
      asm volatile("global_load_dwordx4 %0, %1, %2" : "=v"(e0) : "v"(off), "s"(M ? p.b1 : p.wphi) : "memory");
    }
    unsigned va[5];
#pragma unroll
    for (int i = 0; i < 2; i++) { const int row = 8 * (2 * w + i) + (lane >> 3); va[i] = (rt * 128 + row) * 1024 + (((lane & 7) ^ ((row >> 1) & 7)) * 16); }
    glds(rs_x, w * 2048, va[0], rot * 128);
    glds(rs_x, w * 2048 + 1024, va[1], rot * 128);
    constexpr unsigned wstr = M ? 2752 : 3072, wstep = 64 * wstr;
#pragma unroll
    for (int i = 0; i < 3; i++) {
      const unsigned n = (3 * w + i) * 64 + lane, k = n / 24, cp = n - k * 24;
      const unsigned c = cp ^ ((k & 2) << 1), seg = c >> 3;
      va[2 + i] = M ? k * wstr + u0 * 64 + c * 16 : k * wstr + (seg == 2 ? 0 : seg + 1) * 1024 + grp * 128 + (c & 7) * 16;
      glds(rs_w, WOFF + w * 3072 + i * 1024, va[2 + i], rot * wstep);
    }
    {
      const unsigned n = b * 8 + w;
      glds(__builtin_amdgcn_make_buffer_rsrc((void*)(n < 1376 ? p.w2 : p.wo), 0, n < 1376 ? F * D * 2 : D * D * 2, 0x00020000), SLOT + w * 2048, lane * 16, (n < 1376 ? n : n - 1376) * 1024);
      const unsigned fo = g == 0 ? WS_KVF + (grp * 8 + w) * 128 : WS_HF + (g - 1) * (WS_OF - WS_HF) + (panel * 2 + (w >> 2)) * 128;
      const u64 a = g == 3 ? (u64)p.b2 + ct * 128 : (u64)wsp + fo;
      asm volatile("s_mov_b32 m0, %0\n\ts_nop 0\n\tglobal_load_lds_dword %1, off" ::"s"(SLOT + w * 2048 + 1024), "v"(a + j * 8) : "memory");
    }
    int xb0 = (32 * tg + j32) * 128 + ((h ^ ((j32 >> 1) & 7)) * 16);
    int wb0 = WOFF + (8 * h + q4) * 384 + ((ch * 64 + 32 * (P8 & 1) + 8 * (P8 >> 1)) ^ ((q4 & 2) << 5));
    const int crow = WOFF + (8 * g + q4) * 384;
    int cb0 = crow + (tg >> 1) * 128 + ((ch * 64 + 32 * (tg & 1) + 8 * p4) ^ ((q4 & 2) << 5));
    int cb1 = crow + (cs1 >> 1) * 128 + ((ch * 64 + 32 * (cs1 & 1) + 8 * p4) ^ ((q4 & 2) << 5));
    asm volatile("" : "+v"(xb0), "+v"(wb0));
    if (M) asm volatile("" : "+v"(cb0), "+v"(cb1));
#pragma unroll
    for (int n = 0; n < 3; n++) { acc[n] = zero16; asm volatile("" : "+v"(acc[n])); }
    STAMP(21);
    asm volatile("s_waitcnt vmcnt(7)" : "+v"(e0), "+v"(e1), "+v"(xr), "+v"(ev) :: "memory");
    { u64 z = (((u64)(unsigned)__builtin_amdgcn_readfirstlane(ev[1]) << 32) | (unsigned)__builtin_amdgcn_readfirstlane(ev[0])) + 0x9E3779B97F4A7C15ull;
      z = (z ^ (z >> 30)) * 0xBF58476D1CE4E5B9ull; z = (z ^ (z >> 27)) * 0x94D049BB133111EBull; tag = z ^ (z >> 31); }
    if (!M) {
      LAS char* mine = lds + 3 * SLOT + w * 1024;
      *(LAS v4u*)(mine + lane * 16) = e0;
#pragma unroll
      for (int s = 0; s < 2; s++) wp[s] = trr2(mine + (h * 16 + 8 * s + q4) * 32 + 8 * (P8 >> 1), 128);
    }
    v4u xf[4], wf[4][3], cf[2][2];
    auto t1 = [&](int n) {
      if (n < 2) glds(rs_x, SLOT + w * 2048 + n * 1024, va[n], ((rot + 1) & 7) * 128);
      else glds(rs_w, SLOT + WOFF + w * 3072 + (n - 2) * 1024, va[n], ((rot + 1) & 7) * wstep);
      SB();
    };
    {
      t1(0);
      float sv[3] = {0.f, 0.f, 0.f};
#pragma unroll
      for (int e = 0; e < 4; e++) {
        sv[0] = __builtin_amdgcn_fdot2_f32_bf16(asb2(xr[e]), asb2(0x3F803F80u), sv[0], false);
        sv[1] = __builtin_amdgcn_fdot2_f32_bf16(asb2(xr[e]), asb2(xr[e]), sv[1], false);
        sv[2] = __builtin_amdgcn_fdot2_f32_bf16(asb2(xr[e]), asb2(e1[e]), sv[2], false);
      }
#pragma unroll
      for (int e = 0; e < 3; e++) {
        float v = sv[e];
        v += asf(__builtin_amdgcn_update_dpp(0u, asu(v), 0x121, 0xf, 0xf, false));
        v += asf(__builtin_amdgcn_update_dpp(0u, asu(v), 0x122, 0xf, 0xf, false));
        v += asf(__builtin_amdgcn_update_dpp(0u, asu(v), 0x124, 0xf, 0xf, false));
        sv[e] = v + asf(__builtin_amdgcn_update_dpp(0u, asu(v), 0x128, 0xf, 0xf, false));
      }
      asm volatile("" : "+v"(sv[0]), "+v"(sv[1]), "+v"(sv[2]));
      t1(1);
#pragma unroll
      for (int e = 0; e < 3; e++) sv[e] = sum32(sum16(sv[e]));
      const float mean = sv[0] * (1.f / D);
      const float rstd = __builtin_amdgcn_rsqf(fmaxf(sv[1] * (1.f / D) - mean * mean, 0.f) + 1e-5f);
      const float gate = __builtin_amdgcn_rcpf(1.f + __builtin_amdgcn_exp2f(-1.4426950408889634f * sv[2]));
      float val = lane == 1 ? -mean * rstd : rstd;
      val = lane == 2 ? gate : val;
      __builtin_amdgcn_raw_buffer_store_b64((v2u){asu(val), (unsigned)tag}, rs_ws, lane < 3 ? WS_ST + stok * 32 + lane * 8 : WS_END, 0, AUX_SC1);
      SB();
      t1(2);
    }
    if (!M) {
#pragma unroll
      for (int s = 0; s < 2; s++) {
#pragma unroll
        for (int e = 0; e < 4; e++) wp[s][e] = act ? wp[s][e] : 0u;
        asm volatile("" : "+v"(wp[s]));
      }
    }
    t1(3);
    xf[2] = z4; xf[3] = z4; cf[1][0] = z4; cf[1][1] = z4; cs[0] = zero; cs[1] = zero;
#pragma unroll
    for (int n = 0; n < 3; n++) { wf[2][n] = z4; wf[3][n] = z4; asm volatile("" : "+v"(wf[2][n]), "+v"(wf[3][n])); }
    asm volatile("" : "+v"(xf[2]), "+v"(xf[3]));
    if (M) asm volatile("" : "+v"(cf[1][0]), "+v"(cf[1][1]), "+v"(cs[0]), "+v"(cs[1]));
    t1(4);
    STAMP(13);
    __amdgpu_buffer_rsrc_t ra = rs_x, rb = rs_w;
    unsigned oA = w * 2048, oB = WOFF + w * 3072, dst = 1024, sst = 0, mulB = wstep / 128, addB = 0, sA = ((2 + rot) & 7) * 128, sinc = 128, smsk = 1023;
#pragma nounroll
    for (int i = 0; i < 8; i++) {
      const unsigned so = (i & 3) * SLOT;
      LAS char* wq = lds + so + wb0;
      if (__builtin_expect(i == 6, 0)) {
#pragma unroll
        for (int n = 0; n < 5; n++) va[n] = w2v;
        ra = rs_w2; rb = rs_w2; oA = w * 1024; oB = oA + 16384; dst = 8192; sst = 65536; mulB = 1; addB = 131072; sA = 0; sinc = 327680; smsk = ~0u;
      }
      WAITV(5);
      LBAR();
      auto rx = [&](int s) { xf[s] = *(LAS v4u*)(lds + ((so + xb0) ^ (32 * s))); };
      auto rw = [&](int s, int t) { wf[s][t] = trr2(wq + t * 128 + s * 6144, 1536); };
      auto mm = [&](int s, int t) { acc[t] = mfma32(wf[s][t], xf[s], acc[t]); SB(); };
      auto rc = [&](int ks) { if (M) { cf[ks][0] = trr2(lds + so + cb0 + ks * 12288, 1536); cf[ks][1] = trr2(lds + so + cb1 + ks * 12288, 1536); } };
      auto mc = [&](int ks) { if (M) { cs[0] = mfma16(cf[ks][0], ones, cs[0]); cs[1] = mfma16(cf[ks][1], ones, cs[1]); SB(); } };
      mm(2, 0); rx(0); rw(0, 0);
      const unsigned dsl = ((i + 2) & 3) * SLOT;
      const unsigned sB = sA * mulB + addB, dA = dsl + oA, dB = dsl + oB;
      if (M) { if (__builtin_expect(i == 7, 0)) asm volatile("s_mov_b32 m0, %0\n\ts_nop 0\n\tbuffer_load_dwordx4 %1, %2, %3 offen sc1 lds" ::"s"(STAT + tg * 1024), "v"(lane * 16), "s"(rs_ws), "s"(WS_ST + (rt * 128 + tg * 32) * 32) : "memory"); }
      SB();
      mm(2, 1); rw(0, 1); rw(0, 2); glds(ra, dA, va[0], sA); SB();
      mm(2, 2); rx(1); rw(1, 0); SB();
      mc(1);
      mm(3, 0); rw(1, 1); rw(1, 2); glds(ra, dA + dst, va[1], sA + sst); SB();
      mm(3, 1); rx(2); rw(2, 0); sA = (sA + sinc) & smsk; SB();
      mm(3, 2); rw(2, 1); rw(2, 2); glds(rb, dB, va[2], sB); SB();
      mm(0, 0); rx(3); rw(3, 0); SB();
      mm(0, 1); rw(3, 1); rw(3, 2); glds(rb, dB + dst, va[3], sB + sst); SB();
      mm(0, 2); rc(0); SB();
      mm(1, 0); rc(1); glds(rb, dB + 2 * dst, va[4], sB + 2 * sst); SB();
      mm(1, 1);
      mc(0);
      mm(1, 2);
    }
    rs_wo = __builtin_amdgcn_make_buffer_rsrc((void*)args_again()->wo, 0, D * D * 2, 0x00020000);
    if (M) { cs[0] = mfma16(cf[1][0], ones, cs[0]); cs[1] = mfma16(cf[1][1], ones, cs[1]); }
#pragma unroll
    for (int t = 0; t < 3; t++) { acc[t] = mfma32(wf[2][t], xf[2], acc[t]); acc[t] = mfma32(wf[3][t], xf[3], acc[t]); }
  };

  auto rpiece = [&](int r, int page, int n) {
    if (r < 11) glds(rs_w2, page * PAGE + n * 8192 + w * 1024, w2v, r * 131072 + n * 65536);
    else glds(rs_wo, page * PAGE + n * 8192 + w * 1024, w2v, (r - 11) * 131072 + n * 65536);
  };
  auto round_ld = [&](int r, int page) { rpiece(r, page, 0); rpiece(r, page, 1); };
  const unsigned actw = WS_ACT + (rt * 4 + tg) * 60 * 2048 + lane * 16;

  const int th = w >> 2, q = w & 3;
  const int ab0 = q * 4096 + (16 * h + q4) * 128 + ((32 * blk + 8 * p4) ^ ((q4 & 2) << 5));
  v4u wt[5][2][2];
  auto transit = [&]() {
#pragma unroll
    for (int r = 0; r < 5; r++)
#pragma unroll
      for (int s = 0; s < 2; s++)
#pragma unroll
        for (int c = 0; c < 2; c++) wt[r][s][c] = trr2(lds + r * PAGE + (ab0 ^ (64 * c)) + s * 1024, 512);
  };
  const int tgi = panel * 2 + th;
  const GAS u64* hline = (const GAS u64*)(wsp + WS_HF) + tgi * 16;
  bool hok = false;

  if (isM) {
    phase1(std::true_type{});
    STAMP(1);
    if (j == 0) {
      *(LAS f32x4*)(lds + STAT + 4096 + (ch * 96 + tg * 16 + 4 * g) * 4) = cs[0];
      *(LAS f32x4*)(lds + STAT + 4096 + (ch * 96 + cs1 * 16 + 4 * g) * 4) = cs[1];
    }
    if (j < 6 && blk == 0 && tg == 0) {
      LAS char* d = lds + STAT + 5120 + (ch * 96 + (j >> 1) * 32 + 16 * h + 8 * (j & 1)) * 4;
      *(LAS f32x4*)d = (f32x4){bflo(e0[0]), bfhi(e0[0]), bflo(e0[1]), bfhi(e0[1])};
      *(LAS f32x4*)(d + 16) = (f32x4){bflo(e0[2]), bfhi(e0[2]), bflo(e0[3]), bfhi(e0[3])};
    }
    WAITV(5);
    v4u st = *(LAS v4u*)(lds + STAT + tg * 1024 + j32 * 32);
    LBAR();
    f32x4 c4v[3][4], b4v[3][4];
    auto rcb = [&](int sg) {
      LAS char* cp = lds + STAT + 4096 + (ch * 96 + sg * 32 + 16 * h) * 4;
#pragma unroll
      for (int e4 = 0; e4 < 4; e4++) { c4v[sg][e4] = *(LAS f32x4*)(cp + e4 * 16); b4v[sg][e4] = *(LAS f32x4*)(cp + 1024 + e4 * 16); }
    };
    rcb(0);
    if (!__all(st[1] == (unsigned)tag && st[3] == (unsigned)tag)) {
#pragma nounroll
      for (unsigned spins = 0; spins < SPIN_CAP; ++spins) {
        const GAS u64* gp = (const GAS u64*)(wsp + WS_ST) + (rt * 128 + tg * 32 + j32) * 4;
        const u64 g0 = __hip_atomic_load(gp, RLX_AGENT), g1 = __hip_atomic_load(gp + 1, RLX_AGENT);
        st = (v4u){(unsigned)g0, (unsigned)(g0 >> 32), (unsigned)g1, (unsigned)(g1 >> 32)};
        if (__all(st[1] == (unsigned)tag && st[3] == (unsigned)tag)) break;
      }
    }
    const f32x2 rstd = bc2(asf(st[0])), nmr = bc2(asf(st[2]));
    auto unit = [&](int sg, auto&& j0, auto&& j1, auto&& j2) {
      f32x2 v[8], t[8];
#pragma unroll
      for (int e = 0; e < 8; e++) {
        const f32x2 cc = {c4v[sg][e >> 1][2 * (e & 1)], c4v[sg][e >> 1][2 * (e & 1) + 1]}, bb = {b4v[sg][e >> 1][2 * (e & 1)], b4v[sg][e >> 1][2 * (e & 1) + 1]};
        t[e] = fma2(nmr, cc, bb);
      }
#pragma unroll
      for (int e = 0; e < 8; e++) v[e] = fma2((f32x2){acc[sg][2 * e], acc[sg][2 * e + 1]}, rstd, t[e]);
      PIN8(v); j0(); SB();
#pragma unroll
      for (int e = 0; e < 8; e++) t[e] = v[e] * fma2(v[e] * v[e], bc2(-0.044715f * 2.302208198f), bc2(-2.302208198f));
      PIN8(t); j1(); SB();
#pragma unroll
      for (int e = 0; e < 8; e++) t[e] = (f32x2){__builtin_amdgcn_exp2f(t[e][0]), __builtin_amdgcn_exp2f(t[e][1])} + 1.f;
      PIN8(t); j2(); SB();
      v4u hv[2];
#pragma unroll
      for (int e = 0; e < 8; e++) hv[e >> 2][e & 3] = pk2(v[e] * (f32x2){__builtin_amdgcn_rcpf(t[e][0]), __builtin_amdgcn_rcpf(t[e][1])});
      const int lu = 2 * sg + ch;
      const unsigned so = (lu < 5 || grp < 3) ? (u0 + lu) * 2048 : WS_END;
      __builtin_amdgcn_raw_buffer_store_b128(hv[0], rs_ws, actw, so, AUX_SC1);
      __builtin_amdgcn_raw_buffer_store_b128(hv[1], rs_ws, actw + 1024, so, AUX_SC1);
      SB();
    };
    rcb(1);
    unit(0, [&]() { rpiece(5, 5, 0); }, [&]() { rpiece(5, 5, 1); }, [&]() { rpiece(6, 6, 0); });
    rcb(2);
    unit(1, [&]() { rpiece(6, 6, 1); }, [&]() { rpiece(8, 8, 0); }, [&]() { rpiece(8, 8, 1); });
    unit(2, [&]() { rpiece(9, 9, 0); }, [&]() { rpiece(9, 9, 1); }, [&]() {});
    STAMP(2);
    WAITV(14);
    LBAR();
    transit();
    publish((GAS u64*)(wsp + WS_HF) + ((rt * 4 + tg) * 16 + grp * 2 + ch), tag, lane);
    STAMP(3);
    LBAR();
    round_ld(7, 7); round_ld(10, 0); round_ld(11, 1); round_ld(12, 2);
    STAMP(4);
  } else {
    phase1(std::false_type{});
    STAMP(1);
    LBAR();
    v4u ph[4];
    LAS char* row = lds + IMG + ((2 * ch + h) * 128 + 32 * tg + j32) * 96;
    const f32x16 pkk = mfma32(wp[1], pack8(acc[0], 8), mfma32(wp[0], pack8(acc[0], 0), zero16));
    *(LAS v4u*)(row + 64) = pack8(acc[1], 0);
    *(LAS v4u*)(row + 80) = pack8(acc[1], 8);
    dual_softmax(pkk, ph, [&](int n) { if (n == 2) { *(LAS v4u*)row = ph[0]; *(LAS v4u*)(row + 16) = ph[1]; } rpiece(5 + (n >> 1), 5 + (n >> 1), n & 1); });
    *(LAS v4u*)(row + 32) = ph[2]; *(LAS v4u*)(row + 48) = ph[3];
    LBAR();
    STAMP(22);
    f32x4 kv = zero, zz = zero, kv2 = zero, zz2 = zero;
    LAS char* ib = lds + IMG + ((w >> 1) * 128 + 4 * g + q4) * 96 + 8 * p4;
#pragma unroll
    for (int ks = 0; ks < 4; ks++) {
      const v4u pa = trr2(ib + ks * 32 * 96 + 32 * (w & 1), 16 * 96), vb = trr2(ib + ks * 32 * 96 + 64, 16 * 96);
      if (ks & 1) { kv2 = mfma16(pa, vb, kv2); zz2 = mfma16(pa, ones, zz2); } else { kv = mfma16(pa, vb, kv); zz = mfma16(pa, ones, zz); }
    }
    kv += kv2; zz += zz2;
    const unsigned kvb = WS_KVP + (grp * 8 + w) * 16 * 640;
    {
      const f32x4 sel = (g & 1) ? zz : kv, oth = (g & 1) ? kv : zz;
      const unsigned a0 = pk(sel[0], sel[1]), a1 = pk(sel[2], sel[3]), b0 = pk(oth[0], oth[1]), b1 = pk(oth[2], oth[3]);
      const auto r0 = __builtin_amdgcn_permlane16_swap(b0, b0 ^ 0u, false, false), r1 = __builtin_amdgcn_permlane16_swap(b1, b1 ^ 0u, false, false);
      const unsigned p0 = (g & 1) ? r0[0] : r0[1], p1 = (g & 1) ? r1[0] : r1[1];
      const v4u piece = (g & 1) ? (v4u){p0, p1, a0, a1} : (v4u){a0, a1, p0, p1};
      const unsigned off = (g & 1) ? (j == 0 ? kvb + rt * 640 + 512 + (g >> 1) * 16 : WS_END) : kvb + rt * 640 + ((g >> 1) * 16 + j) * 16;
      __builtin_amdgcn_raw_buffer_store_b128(piece, rs_ws, off, 0, AUX_SC1);
    }
    const f32x16 pkq = mfma32(wp[1], pack8(acc[2], 8), mfma32(wp[0], pack8(acc[2], 0), zero16));
    STAMP(2);
    WAITV(5);
    LBAR();
    transit();
    publish((GAS u64*)(wsp + WS_KVF) + ((grp * 8 + w) * 16 + rt), tag, lane);
    STAMP(3);
    LBAR();
    dual_softmax(pkq, ph, [&](int n) { if (n < 3) round_ld(7 + n, 7 + n); if (n < 2) rpiece(10, 0, n); });
    asm volatile("" : "+v"(ph[0]), "+v"(ph[1]), "+v"(ph[2]), "+v"(ph[3]));
    v4u sela;
#pragma unroll
    for (int e = 0; e < 4; e++) sela[e] = j == 8 * (g >> 1) + 2 * e ? 0x3F80u : j == 8 * (g >> 1) + 2 * e + 1 ? 0x3F800000u : 0u;
    asm volatile("" : "+v"(sela));
    STAMP(4);
    wait16((const GAS u64*)(wsp + WS_KVF) + (grp * 8 + w) * 16, tag, lane);
    STAMP(5);
    v4u pt[8];
#pragma unroll
    for (int r = 0; r < 8; r++) pt[r] = __builtin_amdgcn_raw_buffer_load_b128(rs_ws, kvb + (g & 1) * 8 * 640 + ((g >> 1) * 16 + j) * 16, r * 640, AUX_SC1);
    const v4u zr = __builtin_amdgcn_raw_buffer_load_b128(rs_ws, kvb + ((g & 1) * 8 + (j & 7)) * 640 + 512 + (g >> 1) * 16, 0, AUX_SC1);
    SB();
    kv = zero; kv2 = zero;
#pragma unroll
    for (int r = 0; r < 8; r += 2) { kv = mfma16(sela, pt[r], kv); kv2 = mfma16(sela, pt[r + 1], kv2); }
    f32x4 z = mfma16(sela, zr, zero);
    kv += kv2;
    *(LAS v2u*)(lds + KVR + ((w >> 1) * 17 + j) * 64 + (16 * (w & 1) + 4 * g) * 2) = (v2u){pk(kv[0], kv[1]), pk(kv[2], kv[3])};
#pragma unroll
    for (int e = 0; e < 4; e++) {
      float v = z[e];
      v += asf(__builtin_amdgcn_update_dpp(0u, asu(v), 0x121, 0xf, 0xf, false));
      v += asf(__builtin_amdgcn_update_dpp(0u, asu(v), 0x122, 0xf, 0xf, false));
      v += asf(__builtin_amdgcn_update_dpp(0u, asu(v), 0x124, 0xf, 0xf, false));
      z[e] = v;
    }
    if (j == 0) *(LAS v2u*)(lds + KVR + ((w >> 1) * 17 + 16) * 64 + (16 * (w & 1) + 4 * g) * 2) = (v2u){pk(z[0], z[1]), pk(z[2], z[3])};
    if (tid == 0) *(LAS v4u*)(lds + KVR_ZERO) = z4;
    LBAR();
    STAMP(6);
    const int hb = KVR + (2 * ch + h) * 17 * 64;
    const int ko = act ? hb + (4 * (j32 >> 3) + (j32 & 3)) * 64 : KVR_ZERO, st16 = act ? 16 : 0;
    f32x16 num = zero16;
    float den = 1e-6f, den2 = 0.f;
#pragma unroll
    for (int e = 0; e < 4; e++) {
      num = mfma32(*(LAS v4u*)(lds + ko + e * st16), ph[e], num);
      const v4u zc = *(LAS v4u*)(lds + hb + 16 * 64 + e * 16);
#pragma unroll
      for (int u = 0; u < 4; u += 2) {
        den = __builtin_amdgcn_fdot2_f32_bf16(asb2(zc[u]), asb2(ph[e][u]), den, false);
        den2 = __builtin_amdgcn_fdot2_f32_bf16(asb2(zc[u + 1]), asb2(ph[e][u + 1]), den2, false);
      }
    }
    num *= __builtin_amdgcn_rcpf(den + den2);
    __builtin_amdgcn_raw_buffer_store_b128(pack8(num, 0), rs_ws, actw, (44 + 2 * grp + ch) * 2048, AUX_SC1);
    __builtin_amdgcn_raw_buffer_store_b128(pack8(num, 8), rs_ws, actw + 1024, (44 + 2 * grp + ch) * 2048, AUX_SC1);
    u64 hv;
    asm volatile("global_load_dwordx2 %0, %1, off sc1" : "=v"(hv) : "v"(hline + j) : "memory");
    round_ld(11, 1); round_ld(12, 2);
    asm volatile("s_waitcnt vmcnt(4)" : "+v"(hv) :: "memory");
    if (lane == 0) __hip_atomic_store((GAS u64*)(wsp + WS_OF) + ((rt * 4 + tg) * 16 + grp * 2 + ch), tag, RLX_AGENT);
    hok = __all(hv == tag);
    STAMP(7);
  }

  if (!hok) wait16(hline, tag, lane);
  __builtin_amdgcn_fence(__ATOMIC_ACQUIRE, "wavefront");
  STAMP(8);
  const unsigned av = WS_ACT + (tgi * 60 + q) * 2048 + lane * 16;
  const GAS u64* oline = (const GAS u64*)(wsp + WS_OF) + tgi * 16;
  v4u bf[15][2], b2v; u64 ov; float gl, gt; void* outp; const void* b2p;
  auto ld = [&](int r) {
#pragma unroll
    for (int s = 0; s < 2; s++) bf[r][s] = __builtin_amdgcn_raw_buffer_load_b128(rs_ws, av + s * 1024, (r == 10 && q == 3) ? WS_END : r * 8192, AUX_SC1);
  };
#pragma unroll
  for (int r = 0; r < WIN; r++) ld(r);
  SB();
  if (!isM) LBAR();
  SB();
  f32x16 ob[2] = {zero16, zero16}, oa[2] = {zero16, zero16};
#pragma unroll
  for (int r = 0; r < 15; r++) {
    if (r + WIN == 11) {
      STAMP(9);
      asm volatile("s_waitcnt vmcnt(4)" : "+v"(ov) :: "memory");
      if (!__all(ov == tag)) wait16(oline, tag, lane);
      __builtin_amdgcn_fence(__ATOMIC_ACQUIRE, "wavefront");
      STAMP(10);
      gl = asf(__builtin_amdgcn_raw_buffer_load_b32(rs_ws, WS_ST + (tgi * 32 + j32) * 32 + 16, 0, AUX_SC1));
    }
    if (r + WIN == 9) asm volatile("global_load_dwordx2 %0, %1, off sc1" : "=v"(ov) : "v"(oline + j) : "memory");
    if (r + WIN < 15) ld(r + WIN);
    if (r == 0 || r == 1) round_ld(13 + r, 3 + r);
    if (r == 7) { LBAR(); const KP k = args_again(); outp = k->out; b2p = k->b2; }
    if (r == 9 && b == 0 && w == 7) {
#pragma nounroll
      for (unsigned spins = 0; spins < SPIN_CAP; ++spins) {
        bool ok = true;
#pragma unroll
        for (int i = 0; i < 16; i++) ok &= __hip_atomic_load((const GAS u64*)(wsp + WS_HF) + i * 64 + lane, RLX_AGENT) == tag;
        if (__all(ok)) break;
        __builtin_amdgcn_s_sleep(1);
      }
      if (lane == 0) __hip_atomic_store((GAS u64*)(wsp + WS_E), tag, RLX_AGENT);
    }
    if (r == 10) {
      LBAR();
      gt = asf(__builtin_amdgcn_raw_buffer_load_b32(rs_ws, WS_ST + (panel * 64 + (tid >> 3)) * 32 + 16, 0, AUX_SC1));
      b2v = *(const GAS v4u*)((const GAS char*)b2p + (ct * 64 + (tid & 7) * 8) * 2);
    }
    auto rdw = [&](int rr) {
#pragma unroll
      for (int s = 0; s < 2; s++)
#pragma unroll
        for (int c = 0; c < 2; c++) wt[rr & 1][s][c] = trr2(lds + (rr < 10 ? rr : rr - 10) * PAGE + (ab0 ^ (64 * c)) + s * 1024, 512);
    };
    if (r == 7 || r == 10) rdw(r);
    if (r + 1 >= 5 && r + 1 < 15 && r + 1 != 7 && r + 1 != 10) rdw(r + 1);
    SB();
#pragma unroll
    for (int s = 0; s < 2; s++)
#pragma unroll
      for (int c = 0; c < 2; c++) {
        const v4u wf = wt[r < 5 ? r : r & 1][s][c];
        if (r < 11) ob[c] = mfma32(wf, bf[r][s], ob[c]); else oa[c] = mfma32(wf, bf[r][s], oa[c]);
      }
    SB();
  }
  STAMP(11);
#pragma unroll
  for (int c = 0; c < 2; c++) {
    const f32x16 v = ob[c] + gl * (oa[c] - ob[c]);
#pragma unroll
    for (int rg = 0; rg < 4; rg++)
      *(LAS f32x4*)(lds + RED + (q * 64 + 32 * th + j32) * 256 + (((8 * c + 2 * rg + h) ^ (j32 & 7)) * 16)) = (f32x4){v[4 * rg], v[4 * rg + 1], v[4 * rg + 2], v[4 * rg + 3]};
  }
  LBAR();
  {
    const int tok = tid >> 3, c8 = tid & 7;
    const float og = 1.f - gt;
    f32x4 s0 = (f32x4){bflo(b2v[0]), bfhi(b2v[0]), bflo(b2v[1]), bfhi(b2v[1])} * og, s1 = (f32x4){bflo(b2v[2]), bfhi(b2v[2]), bflo(b2v[3]), bfhi(b2v[3])} * og;
#pragma unroll
    for (int qq = 0; qq < 4; qq++) {
      s0 += *(LAS f32x4*)(lds + RED + (qq * 64 + tok) * 256 + (((2 * c8) ^ (tok & 7)) * 16));
      s1 += *(LAS f32x4*)(lds + RED + (qq * 64 + tok) * 256 + (((2 * c8 + 1) ^ (tok & 7)) * 16));
    }
    __builtin_amdgcn_raw_buffer_store_b128((v4u){pk(s0[0], s0[1]), pk(s0[2], s0[3]), pk(s1[0], s1[1]), pk(s1[2], s1[3])},
                                           __builtin_amdgcn_make_buffer_rsrc(outp, 0, S * D * 2, 0x00020000), ((panel * 64 + tok) * 512 + ct * 64 + c8 * 8) * 2, 0, OUT_AUX);
  }
  STAMP(12);
}
}

extern "C" void kernel_launch(void* const* d_in, const int* in_sizes, int n_in, void* d_out, int out_size, void* d_ws, size_t ws_size,
                              hipStream_t stream) {
  gm::Params p;
  p.x = d_in[0]; p.wqkv = d_in[1]; p.wphi = d_in[2]; p.wo = d_in[3];
  p.w1 = d_in[5]; p.b1 = d_in[6]; p.w2 = d_in[7]; p.b2 = d_in[8]; p.gw = d_in[9];
  p.out = d_out; p.ws = d_ws;
  gm::fused<<<256, 512, 0, stream>>>(p);
}
```
